```python
import torch

def _softmax(x, axis=-1):
    return torch.softmax(x, dim=axis)
def _gelu(x):
    return torch.nn.functional.gelu(x, approximate="tanh")
def _silu(x):
    return torch.nn.functional.silu(x)
def _sigmoid(x):
    return torch.sigmoid(x)
def _layernorm(x, axis=-1, eps=1e-5):
    m = x.mean(dim=axis, keepdim=True)
    v = x.var(dim=axis, keepdim=True, unbiased=False)
    return (x - m) / torch.sqrt(v + eps)
def _scatter_add(tgt, idx, src, axis=0):
    return tgt.clone().index_add_(axis, idx, src)
def _segment_sum(src, seg, n):
    out = torch.zeros((n,) + src.shape[1:], dtype=src.dtype)
    return out.index_add_(0, seg, src)
def _pad(x, pw):
    flat = tuple(v for pair in reversed(pw) for v in pair)
    return torch.nn.functional.pad(x, flat)

kernel_name = 'standalone__mixer_channel_mlp__prompt'

def setup_inputs():

    _w = (lambda a: (lambda t: t.to(torch.bfloat16) if t.is_floating_point() else t)(torch.as_tensor(a)))
    B, S, D, F = 1, 2048, 640, 1536
    return {
        "x": _w((lambda *s: torch.randn(*s, dtype=torch.bfloat16) * 0.05)(B, S, D)),
        "w1": _w((lambda *s: torch.randn(*s, dtype=torch.bfloat16) * 0.8)(D, F) * (D ** -0.5)),
        "b1": _w((lambda *s: torch.randn(*s, dtype=torch.bfloat16) * 0.4)(F)),
        "w2": _w((lambda *s: torch.randn(*s, dtype=torch.bfloat16) * 0.05)(F, D) * (F ** -0.5)),
        "b2": _w((lambda *s: torch.randn(*s, dtype=torch.bfloat16) * 0.05)(D)),
    }

def reference(x, w1, b1, w2, b2):
    y = _layernorm(x, axis=-1)
    h = _gelu(torch.einsum("bsd,df->bsf", y, w1) + b1)
    return torch.einsum("bsf,fd->bsd", h, w2) + b2
```

```cpp
#include <hip/hip_runtime.h>
#include <hip/hip_bf16.h>
#include <stdint.h>

typedef __bf16 bf16x2 __attribute__((ext_vector_type(2)));
typedef __bf16 bf16x8 __attribute__((ext_vector_type(8)));
typedef short s16x4 __attribute__((ext_vector_type(4)));
typedef short s16x8 __attribute__((ext_vector_type(8)));
typedef float f32x2 __attribute__((ext_vector_type(2)));
typedef float f32x4 __attribute__((ext_vector_type(4)));
typedef unsigned u32x2 __attribute__((ext_vector_type(2)));
typedef unsigned u32x4 __attribute__((ext_vector_type(4)));
#define LAS __attribute__((address_space(3)))
#define GAS __attribute__((address_space(1)))
#define DI __device__ __forceinline__

constexpr int S_ = 2048, D_ = 640, F_ = 1536;

#define WAIT_V(n) asm volatile("s_waitcnt vmcnt(" #n ")" ::: "memory")
#define WAIT_L0() asm volatile("s_waitcnt lgkmcnt(0)" ::: "memory")
#define BAR() asm volatile("s_barrier" ::: "memory")

DI void glds16(unsigned voff, const char* sbase, unsigned lds) {
  unsigned keep;
  asm volatile("s_mov_b32 %0, m0\n\ts_mov_b32 m0, %3\n\ts_nop 0\n\tglobal_load_lds_dwordx4 %1, %2\n\ts_mov_b32 m0, %0"
               : "=&s"(keep) : "v"(voff), "s"(sbase), "s"(lds) : "memory");
}
DI f32x4 mfma16(s16x8 a, s16x8 b, f32x4 c) {
  return __builtin_amdgcn_mfma_f32_16x16x32_bf16(__builtin_bit_cast(bf16x8, a), __builtin_bit_cast(bf16x8, b), c, 0, 0, 0);
}
DI s16x4 trread(LAS char* p) { return __builtin_amdgcn_ds_read_tr16_b64_v4i16((LAS s16x4*)p); }
DI float dot2(unsigned a, unsigned b, float c) {
  return __builtin_amdgcn_fdot2_f32_bf16(__builtin_bit_cast(bf16x2, a), __builtin_bit_cast(bf16x2, b), c, false);
}
DI unsigned pack2(float lo, float hi) {
  f32x2 v = {lo, hi};
  return __builtin_bit_cast(unsigned, __builtin_convertvector(v, bf16x2));
}
DI float gelu_tanh(float v) {
  constexpr float C1 = -2.0f * 0.7978845608028654f * 1.4426950408889634f;
  constexpr float C2 = C1 * 0.044715f;
  float z = v * __builtin_fmaf(v * v, C2, C1);
  float e = __builtin_amdgcn_exp2f(z);
  return v * __builtin_amdgcn_rcpf(1.0f + e);
}

constexpr int SLOT = 16384;
constexpr int K1_SCR = 9 * SLOT;
constexpr int K1_LDS = 9 * SLOT + 4096;

__global__ __launch_bounds__(512, 2) void k1(const __hip_bfloat16* __restrict__ xg, const __hip_bfloat16* __restrict__ w1g,
                                             const __hip_bfloat16* __restrict__ b1g, u32x4* __restrict__ hg) {
  extern __shared__ __attribute__((aligned(16))) char smem_[];
  LAS char* L = (LAS char*)smem_;
  const unsigned lb = (unsigned)(uintptr_t)L;
  const int tid = threadIdx.x, lane = tid & 63;
  const int w = __builtin_amdgcn_readfirstlane(tid >> 6);
  const int xcd = blockIdx.x & 7, idx = blockIdx.x >> 3;
  const int rb = 8 * (xcd >> 1) + (idx & 7), cc = 4 * (xcd & 1) + (idx >> 3);
  const int rt = w >> 1, pr = w & 1;
  const int m = lane & 15, g = lane >> 4, q = (lane & 15) >> 2, p = lane & 3;

  const char* xbase = (const char*)xg + (size_t)(64 * rb + 8 * w) * 1280;
  const unsigned xv0 = (lane >> 4) * 1280 + (((lane & 15) ^ ((8 * w + (lane >> 4)) & 15)) << 4);
  const unsigned xv1 = ((lane >> 4) + 4) * 1280 + (((lane & 15) ^ ((8 * w + (lane >> 4) + 4) & 15)) << 4);
  const char* wbase = (const char*)w1g + (size_t)(8 * w) * 3072 + (size_t)cc * 384;
  const unsigned wv = (lane >> 3) * 3072 + (((lane & 7) ^ (((lane >> 4) & 1) << 1) ^ ((w & 1) << 2)) << 4);
  auto issueX = [&](int j, int slot) {
    glds16(xv0, xbase + j * 256, lb + slot * SLOT + (2 * w) * 1024);
    glds16(xv1, xbase + j * 256, lb + slot * SLOT + (2 * w + 1) * 1024);
  };
  auto issueW = [&](int t, int slot) {
    const int c = t / 5, j = t % 5;
    const char* s = wbase + (size_t)(128 * j) * 3072 + c * 128;
    glds16(wv, s, lb + slot * SLOT + w * 1024);
    glds16(wv, s + (size_t)64 * 3072, lb + slot * SLOT + (w + 8) * 1024);
  };
  if (lane < 3) glds16(lane * 16, (const char*)b1g + cc * 384 + w * 48, lb + K1_SCR + w * 48);
  issueX(0, 0); issueW(0, 5); issueX(1, 1); issueW(1, 6); issueX(2, 2); issueW(2, 7); issueX(3, 3); issueW(3, 8); issueX(4, 4);

  int xoff[4], woff[2][4];
#pragma unroll
  for (int i = 0; i < 4; ++i) {
    const int ks = (i + rt) & 3;
    xoff[i] = ((16 * rt + m) * 16 + ((4 * ks + g) ^ m)) * 16;
#pragma unroll
    for (int e = 0; e < 2; ++e) {
      int c = (2 * (2 * pr + e) + (p >> 1)) ^ (((q >> 1) & 1) << 1) ^ ((g & 1) << 2);
      woff[e][i] = ks * 4096 + g * 1024 + (8 * q + c) * 16 + (p & 1) * 8;
    }
  }

  s16x8 xf[20];
  f32x4 acc[3][2];
#pragma unroll
  for (int c = 0; c < 3; ++c) { acc[c][0] = f32x4{0, 0, 0, 0}; acc[c][1] = f32x4{0, 0, 0, 0}; }
  float rsum = 0.f, rsq = 0.f;
  float cs[2] = {0.f, 0.f};
  float mean_r = 0.f, rstd = 0.f;
  const unsigned ONES = 0x3f803f80u;

  constexpr int wslot[15] = {5, 6, 7, 8, 0, 5, 1, 6, 2, 7, 3, 8, 4, 0, 5};

  auto epilogue = [&](int c) {
    const LAS float* csp = (const LAS float*)(L + K1_SCR + 512);
    const LAS unsigned short* bp = (const LAS unsigned short*)(L + K1_SCR);
    float hv[2][4];
#pragma unroll
    for (int e = 0; e < 2; ++e) {
      const int n = 64 * c + 16 * (2 * pr + e) + 4 * g;
      f32x4 s0 = *(const LAS f32x4*)(csp + 0 * 192 + n);
      f32x4 s1 = *(const LAS f32x4*)(csp + 1 * 192 + n);
      f32x4 s2 = *(const LAS f32x4*)(csp + 2 * 192 + n);
      f32x4 s3 = *(const LAS f32x4*)(csp + 3 * 192 + n);
      f32x4 csum = (s0 + s1) + (s2 + s3);
      u32x2 bb = *(const LAS u32x2*)(bp + n);
      float bv[4] = {__builtin_bit_cast(float, bb.x << 16), __builtin_bit_cast(float, bb.x & 0xffff0000u),
                     __builtin_bit_cast(float, bb.y << 16), __builtin_bit_cast(float, bb.y & 0xffff0000u)};
#pragma unroll
      for (int j = 0; j < 4; ++j) {
        float t = __builtin_fmaf(-mean_r, csum[j], bv[j]);
        float v = __builtin_fmaf(acc[c][e][j], rstd, t);
        hv[e][j] = gelu_tanh(v);
      }
    }
    u32x4 o = {pack2(hv[0][0], hv[0][1]), pack2(hv[0][2], hv[0][3]), pack2(hv[1][0], hv[1][1]), pack2(hv[1][2], hv[1][3])};
    const int ks = 6 * cc + 2 * c + pr;
    hg[(size_t)((4 * rb + rt) * 48 + ks) * 64 + lane] = o;
  };

#define K1_COMPUTE(t)                                                                          \
  {                                                                                            \
    constexpr int c = (t) / 5, j = (t) % 5;                                                    \
    LAS char* wb = L + wslot[t] * SLOT;                                                        \
    _Pragma("unroll") for (int i = 0; i < 4; ++i) {                                            \
      if (c == 0) {                                                                            \
        xf[4 * j + i] = *(const LAS s16x8*)(L + j * SLOT + xoff[i]);                           \
        u32x4 xv = __builtin_bit_cast(u32x4, xf[4 * j + i]);                                   \
        _Pragma("unroll") for (int z = 0; z < 4; ++z) { rsum = dot2(xv[z], ONES, rsum); rsq = dot2(xv[z], xv[z], rsq); } \
      }                                                                                        \
      _Pragma("unroll") for (int e = 0; e < 2; ++e) {                                          \
        s16x4 lo = trread(wb + woff[e][i]);                                                    \
        s16x4 hi = trread(wb + woff[e][i] + 512);                                              \
        s16x8 wf = __builtin_shufflevector(lo, hi, 0, 1, 2, 3, 4, 5, 6, 7);                    \
        acc[c][e] = mfma16(wf, xf[4 * j + i], acc[c][e]);                                      \
        if (i == 0) {                                                                          \
          u32x4 wq = __builtin_bit_cast(u32x4, wf);                                            \
          _Pragma("unroll") for (int z = 0; z < 4; ++z) cs[e] = dot2(wq[z], ONES, cs[e]);      \
        }                                                                                      \
      }                                                                                        \
    }                                                                                          \
    if (j == 4) {                                                                              \
      _Pragma("unroll") for (int e = 0; e < 2; ++e) {                                          \
        float v = cs[e];                                                                       \
        v += __shfl_xor(v, 16); v += __shfl_xor(v, 32);                                        \
        if (lane < 16) ((LAS float*)(L + K1_SCR + 512))[rt * 192 + 64 * c + 16 * (2 * pr + e) + lane] = v; \
        cs[e] = 0.f;                                                                           \
      }                                                                                        \
      if (c == 0) {                                                                            \
        float s1 = rsum, s2 = rsq;                                                             \
        s1 += __shfl_xor(s1, 16); s1 += __shfl_xor(s1, 32);                                    \
        s2 += __shfl_xor(s2, 16); s2 += __shfl_xor(s2, 32);                                    \
        float mean = s1 * (1.0f / 640.0f);                                                     \
        float var = __builtin_fmaf(-mean, mean, s2 * (1.0f / 640.0f));                         \
        var = var < 0.f ? 0.f : var;                                                           \
        rstd = 1.0f / __builtin_sqrtf(var + 1e-5f);                                            \
        mean_r = mean * rstd;                                                                  \
      }                                                                                        \
    }                                                                                          \
  }

  WAIT_V(14); BAR();
  K1_COMPUTE(0); WAIT_L0();
  WAIT_V(10); BAR(); issueW(4, 0); issueW(5, 5);
  K1_COMPUTE(1); WAIT_L0();
  WAIT_V(10); BAR(); issueW(6, 1); issueW(7, 6);
  K1_COMPUTE(2); WAIT_L0();
  WAIT_V(10); BAR(); issueW(8, 2); issueW(9, 7);
  K1_COMPUTE(3); WAIT_L0();
  WAIT_V(10); BAR(); issueW(10, 3); issueW(11, 8);
  K1_COMPUTE(4); WAIT_L0();
  WAIT_V(12); BAR(); issueW(12, 4); issueW(13, 0);
  epilogue(0);
  K1_COMPUTE(5); WAIT_L0();
  WAIT_V(15); BAR(); issueW(14, 5);
  K1_COMPUTE(6); WAIT_L0();
  WAIT_V(15); BAR();
  K1_COMPUTE(7); WAIT_L0();
  WAIT_V(13); BAR();
  K1_COMPUTE(8); WAIT_L0();
  WAIT_V(11); BAR();
  K1_COMPUTE(9); WAIT_L0();
  WAIT_V(9); BAR();
  epilogue(1);
  K1_COMPUTE(10); WAIT_L0();
  WAIT_V(8); BAR();
  K1_COMPUTE(11); WAIT_L0();
  WAIT_V(6); BAR();
  K1_COMPUTE(12); WAIT_L0();
  WAIT_V(4); BAR();
  K1_COMPUTE(13); WAIT_L0();
  WAIT_V(1); BAR();
  K1_COMPUTE(14); WAIT_L0();
  BAR();
  epilogue(2);
}

constexpr int K2_LDS = 8 * SLOT;

__global__ __launch_bounds__(512, 2) void k2(const u32x4* __restrict__ hg, const __hip_bfloat16* __restrict__ w2g,
                                             const __hip_bfloat16* __restrict__ b2g, __hip_bfloat16* __restrict__ outg) {
  extern __shared__ __attribute__((aligned(16))) char smem_[];
  LAS char* L = (LAS char*)smem_;
  const unsigned lb = (unsigned)(uintptr_t)L;
  const int tid = threadIdx.x, lane = tid & 63;
  const int w = __builtin_amdgcn_readfirstlane(tid >> 6);
  const int xcd = blockIdx.x & 7, idx = blockIdx.x >> 3;
  const int rbg = xcd >> 1;
  const int rbase = rbg * 6 - (rbg > 2 ? 1 : 0);
  const int rcount = rbg < 2 ? 6 : 5;
  const int rl = idx / 5;
  if (rl >= rcount) return;
  const int rb2 = rbase + rl, line = 5 * (xcd & 1) + idx % 5;
  const int kq = w >> 1, mh = w & 1;
  const int m = lane & 15, g = lane >> 4, q = (lane & 15) >> 2, p = lane & 3;

  const char* wbase = (const char*)w2g + (size_t)(384 * kq + 64 * mh) * 1280 + line * 128;
  unsigned wv;
  {
    int r = lane >> 3, a = r >> 1;
    int c = (lane & 7) ^ ((a & 1) | ((a >> 1) << 2));
    wv = r * 1280 + c * 16;
  }
  auto issueW = [&](int s, int slot) {
#pragma unroll
    for (int i = 0; i < 8; ++i)
      glds16(wv, wbase + (size_t)(128 * s + 8 * i) * 1280, lb + slot * SLOT + (8 * mh + i) * 1024);
  };
  issueW(0, kq);
  issueW(1, 4 + kq);
  const int rt0 = 6 * rb2 + 3 * mh;
  u32x4 hf[12][3];
#pragma unroll
  for (int ks = 0; ks < 12; ++ks)
#pragma unroll
    for (int r = 0; r < 3; ++r) {
      int rt = rt0 + r; rt = rt > 127 ? 127 : rt;
      const char* sb = (const char*)(hg + (size_t)(rt * 48 + 12 * kq + ks) * 64);
      asm volatile("global_load_dwordx4 %0, %1, %2" : "=v"(hf[ks][r]) : "v"(lane * 16), "s"(sb) : "memory");
    }
  int woff[4];
  {
    int r = 4 * (g & 1) + q, a = r >> 1, f = (a & 1) | ((a >> 1) << 2);
#pragma unroll
    for (int t = 0; t < 4; ++t)
      woff[t] = (g >> 1) * 1024 + (8 * r + ((4 * (t >> 1) + p) ^ f)) * 16 + (((t & 1) ^ (p & 1)) * 8);
  }
  f32x4 acc[3][4];
#pragma unroll
  for (int r = 0; r < 3; ++r)
#pragma unroll
    for (int t = 0; t < 4; ++t) acc[r][t] = f32x4{0, 0, 0, 0};

#define K2_COMPUTE(s, slot)                                                                    \
  {                                                                                            \
    LAS char* wb = L + (slot) * SLOT;                                                          \
    _Pragma("unroll") for (int k4 = 0; k4 < 4; ++k4) {                                         \
      _Pragma("unroll") for (int t = 0; t < 4; ++t) {                                          \
        LAS char* a0 = wb + woff[t] + k4 * 4096;                                               \
        s16x4 lo = trread(a0);                                                                 \
        s16x4 hi = trread(a0 + 2048);                                                          \
        s16x8 wf = __builtin_shufflevector(lo, hi, 0, 1, 2, 3, 4, 5, 6, 7);                    \
        _Pragma("unroll") for (int r = 0; r < 3; ++r)                                          \
          acc[r][t] = mfma16(wf, __builtin_bit_cast(s16x8, hf[4 * (s) + k4][r]), acc[r][t]);   \
      }                                                                                        \
    }                                                                                          \
  }
#define PIN_H(s) _Pragma("unroll") for (int k4 = 0; k4 < 4; ++k4) _Pragma("unroll") for (int r = 0; r < 3; ++r) asm volatile("" : "+v"(hf[4 * (s) + k4][r]));

  WAIT_V(24); PIN_H(0); BAR();
  K2_COMPUTE(0, kq); WAIT_L0();
  BAR();
  issueW(2, kq);
  WAIT_V(20); PIN_H(1);
  K2_COMPUTE(1, 4 + kq);
  WAIT_V(0); PIN_H(2); WAIT_L0(); BAR();
  K2_COMPUTE(2, kq); WAIT_L0();
  BAR();
  LAS f32x4* red = (LAS f32x4*)L;
#pragma unroll
  for (int r = 0; r < 3; ++r)
#pragma unroll
    for (int t = 0; t < 4; ++t) red[((mh * 4 + kq) * 12 + r * 4 + t) * 64 + lane] = acc[r][t];
  WAIT_L0(); BAR();
#pragma unroll
  for (int pi = 0; pi < 2; ++pi) {
    const int pair = kq + 4 * pi;
    if (pair < 6) {
      const int r = pair >> 1, v = pair & 1;
      f32x4 s0 = {0, 0, 0, 0}, s1 = {0, 0, 0, 0};
#pragma unroll
      for (int k = 0; k < 4; ++k) {
        s0 += red[((mh * 4 + k) * 12 + r * 4 + 2 * v) * 64 + lane];
        s1 += red[((mh * 4 + k) * 12 + r * 4 + 2 * v + 1) * 64 + lane];
      }
      f32x4 lo = (g & 1) ? s1 : s0, hi = (g & 1) ? s0 : s1;
      const int d0 = 64 * line + 32 * v + 8 * g;
      u32x4 bb = *(const u32x4*)(b2g + d0);
      float o[8];
#pragma unroll
      for (int j = 0; j < 4; ++j) { o[j] = lo[j]; o[4 + j] = hi[j]; }
#pragma unroll
      for (int j = 0; j < 4; ++j) {
        o[2 * j] += __builtin_bit_cast(float, bb[j] << 16);
        o[2 * j + 1] += __builtin_bit_cast(float, bb[j] & 0xffff0000u);
      }
      const int rt = rt0 + r;
      if (rt < 128) {
        u32x4 ov = {pack2(o[0], o[1]), pack2(o[2], o[3]), pack2(o[4], o[5]), pack2(o[6], o[7])};
        *(u32x4*)(outg + (size_t)(16 * rt + m) * 640 + d0) = ov;
      }
    }
  }
}

extern "C" void kernel_launch(void* const* d_in, const int* in_sizes, int n_in, void* d_out, int out_size, void* d_ws,
                              size_t ws_size, hipStream_t stream) {
  const __hip_bfloat16* x = (const __hip_bfloat16*)d_in[0];
  const __hip_bfloat16* w1 = (const __hip_bfloat16*)d_in[1];
  const __hip_bfloat16* b1 = (const __hip_bfloat16*)d_in[2];
  const __hip_bfloat16* w2 = (const __hip_bfloat16*)d_in[3];
  const __hip_bfloat16* b2 = (const __hip_bfloat16*)d_in[4];
  u32x4* h = (u32x4*)d_ws;
  (void)hipFuncSetAttribute((const void*)k1, hipFuncAttributeMaxDynamicSharedMemorySize, K1_LDS);
  (void)hipFuncSetAttribute((const void*)k2, hipFuncAttributeMaxDynamicSharedMemorySize, K2_LDS);
  k1<<<256, 512, K1_LDS, stream>>>(x, w1, b1, h);
  k2<<<240, 512, K2_LDS, stream>>>(h, w2, b2, (__hip_bfloat16*)d_out);
}
```

```cpp
#include <hip/hip_runtime.h>
#include <hip/hip_bf16.h>
#include <stdint.h>

typedef __bf16 bf16x2 __attribute__((ext_vector_type(2)));
typedef __bf16 bf16x8 __attribute__((ext_vector_type(8)));
typedef short s16x4 __attribute__((ext_vector_type(4)));
typedef short s16x8 __attribute__((ext_vector_type(8)));
typedef float f32x2 __attribute__((ext_vector_type(2)));
typedef float f32x4 __attribute__((ext_vector_type(4)));
typedef float f32x16 __attribute__((ext_vector_type(16)));
typedef unsigned u32x2 __attribute__((ext_vector_type(2)));
typedef unsigned u32x4 __attribute__((ext_vector_type(4)));
typedef unsigned long long u64;
#define LAS __attribute__((address_space(3)))
#define GAS __attribute__((address_space(1)))
#define DI __device__ __forceinline__

#define WAIT_V(n) asm volatile("s_waitcnt vmcnt(" #n ")" ::: "memory")
#define WAIT_L0() asm volatile("s_waitcnt lgkmcnt(0)" ::: "memory")
#define BAR() asm volatile("s_barrier" ::: "memory")
#define WAITBAR(n) asm volatile("s_waitcnt vmcnt(" #n ") lgkmcnt(0)\n\ts_barrier" ::: "memory")
#define SCHED() __builtin_amdgcn_sched_barrier(0)
#define NOP (void)0

#define STAMP(i)
#define STAMP_DECL
#define STAMP_OUT(base, n)

DI void glds16(unsigned voff, const char* sbase, unsigned lds) {
  unsigned keep;
  asm volatile("s_mov_b32 %0, m0\n\ts_mov_b32 m0, %3\n\ts_nop 0\n\tglobal_load_lds_dwordx4 %1, %2\n\ts_mov_b32 m0, %0"
               : "=&s"(keep) : "v"(voff), "s"(sbase), "s"(lds) : "memory");
}
DI f32x4 mfma16(s16x8 a, s16x8 b, f32x4 c) {
  return __builtin_amdgcn_mfma_f32_16x16x32_bf16(__builtin_bit_cast(bf16x8, a), __builtin_bit_cast(bf16x8, b), c, 0, 0, 0);
}
DI f32x16 mfma32(s16x8 a, s16x8 b, f32x16 c) {
  return __builtin_amdgcn_mfma_f32_32x32x16_bf16(__builtin_bit_cast(bf16x8, a), __builtin_bit_cast(bf16x8, b), c, 0, 0, 0);
}
DI float xsum32(float v) {
  unsigned a = __builtin_bit_cast(unsigned, v);
  auto r = __builtin_amdgcn_permlane32_swap(a, a, false, false);
  unsigned r0 = r[0], r1 = r[1];
  return __builtin_bit_cast(float, r0) + __builtin_bit_cast(float, r1);
}
DI s16x4 trread(LAS char* p) { return __builtin_amdgcn_ds_read_tr16_b64_v4i16((LAS s16x4*)p); }
DI s16x8 trfrag(LAS char* p, int second) {
  s16x4 lo = trread(p), hi = trread(p + second);
  return __builtin_shufflevector(lo, hi, 0, 1, 2, 3, 4, 5, 6, 7);
}
DI unsigned pack2(float lo, float hi) {
  f32x2 v = {lo, hi};
  return __builtin_bit_cast(unsigned, __builtin_convertvector(v, bf16x2));
}
DI unsigned gelu2(f32x2 v) {
  constexpr float C1 = -2.0f * 0.7978845608028654f * 1.4426950408889634f;
  constexpr float C2 = C1 * 0.044715f;
  f32x2 u = v * v;
  f32x2 t = __builtin_elementwise_fma(u, f32x2{C2, C2}, f32x2{C1, C1});
  f32x2 z = v * t;
  f32x2 e = {__builtin_amdgcn_exp2f(z.x), __builtin_amdgcn_exp2f(z.y)};
  f32x2 d = e + f32x2{1.0f, 1.0f};
  f32x2 r = {__builtin_amdgcn_rcpf(d.x), __builtin_amdgcn_rcpf(d.y)};
  f32x2 o = v * r;
  return __builtin_bit_cast(unsigned, __builtin_convertvector(o, bf16x2));
}

constexpr int SLOT = 16384;
constexpr int P1_RED = 8 * SLOT;
constexpr int P1_SCR = 9 * SLOT;
constexpr int LDS_BYTES = 9 * SLOT + 8192;
constexpr size_t WS_EPOCH = 0;
constexpr size_t WS_FLAGS = 4096;
constexpr size_t WS_H = 1u << 20;
constexpr u64 EPOCH_STEP = 0x9E3779B97F4A7C15ull;
constexpr unsigned SPIN_CAP = 1u << 20;

__global__ __launch_bounds__(512, 2) void mixer_fused(const __hip_bfloat16* __restrict__ xg, const __hip_bfloat16* __restrict__ w1g,
                                                      const __hip_bfloat16* __restrict__ b1g, const __hip_bfloat16* __restrict__ w2g,
                                                      const __hip_bfloat16* __restrict__ b2g, __hip_bfloat16* __restrict__ outg,
                                                      char* __restrict__ ws) {
  STAMP_DECL
  extern __shared__ __attribute__((aligned(16))) char smem_[];
  LAS char* L = (LAS char*)smem_;
  const unsigned lb = (unsigned)(uintptr_t)L;
  const int tid = threadIdx.x, lane = tid & 63;
  const int w = __builtin_amdgcn_readfirstlane(tid >> 6);
  const int xcd = blockIdx.x & 7, idx = blockIdx.x >> 3;
  const int m = lane & 15, g = lane >> 4, q = (lane & 15) >> 2, p = lane & 3;
  const unsigned l16 = lane * 16;
  char* hg = ws + WS_H;

  u64 tag;
  asm volatile("global_load_dwordx2 %0, %1, %2 sc1" : "=v"(tag) : "v"(0), "s"(ws + WS_EPOCH) : "memory");

  const int rb = 8 * (xcd >> 1) + (idx & 7), cc = 4 * (xcd & 1) + (idx >> 3);
  const int mh = w & 1, pr = (w >> 1) & 1, kh = w >> 2;
  const int rbg = xcd >> 1;
  const int rbase = rbg * 6 - (rbg > 2 ? 1 : 0);
  const int rl = idx / 5;
  const bool has2 = rl < (rbg < 2 ? 6 : 5);
  const int rb2 = has2 ? rbase + rl : 0, line = 5 * (xcd & 1) + idx % 5;
  const int kq = w >> 1, mh2 = w & 1;

  const char* xbase = (const char*)xg + (size_t)(64 * rb + 8 * w) * 1280;
  const unsigned xv = (lane >> 3) * 1280 + (((lane & 7) ^ ((4 * w + (lane >> 4)) & 7)) << 4);
  const char* wbase = (const char*)w1g + (size_t)(8 * w) * 3072 + (size_t)cc * 384;
  const unsigned wv = (lane >> 3) * 3072 + (((lane & 7) ^ (((lane >> 4) & 1) << 2)) << 4);
  const char* vbase = (const char*)w2g + (size_t)(8 * w) * 1280 + line * 128;
  const unsigned vv = has2 ? (lane >> 3) * 1280 + (((lane & 7) ^ ((lane >> 4) << 1)) << 4) : 0u;
  auto issueX1 = [&](int tau, int slot, int half) {
    glds16(xv, xbase + tau * 128 + half * 640, lb + slot * SLOT + (w + 8 * half) * 1024);
  };
  auto issueW1 = [&](int t, int slot, int half) {
    const int c = t / 5, tau = t % 5;
    glds16(wv, wbase + (size_t)(64 * tau + 320 * half) * 3072 + c * 128, lb + slot * SLOT + (w + 8 * half) * 1024);
  };
  auto issueV1 = [&](int kq_, int s, int slot, int half) {
    glds16(vv, vbase + (size_t)(384 * kq_ + 64 * s + 192 * half) * 1280, lb + slot * SLOT + (w + 8 * half) * 1024);
  };
  issueX1(0, 0, 0); issueX1(0, 0, 1); issueW1(0, 1, 0); issueW1(0, 1, 1); issueX1(1, 2, 0); issueX1(1, 2, 1); issueW1(1, 3, 0); issueW1(1, 3, 1);
  if (lane < 3) glds16(lane * 16, (const char*)b1g + cc * 384 + w * 48, lb + P1_SCR + w * 48);
  issueX1(2, 4, 0); issueX1(2, 4, 1); issueW1(2, 5, 0); issueW1(2, 5, 1); issueX1(3, 6, 0); issueX1(3, 6, 1); issueW1(3, 7, 0); issueW1(3, 7, 1);

  const int hh = lane >> 5, m32 = lane & 31;
  int xoff[4], woff[4];
#pragma unroll
  for (int i = 0; i < 4; ++i) {
    const int ks = (i + 2 * mh + pr) & 3;
    const int row = 32 * mh + m32;
    xoff[i] = kh * 8192 + (8 * row + ((2 * ks + hh) ^ ((row >> 1) & 7))) * 16;
    const int c = (4 * pr + 2 * ((g & 1) ^ kh) + (p >> 1)) ^ (((q >> 1) & 1) << 2);
    woff[i] = kh * 8192 + (2 * ks + hh) * 1024 + (8 * q + c) * 16 + (p & 1) * 8;
  }

  s16x8 xf[20];
  s16x8 wf[2][4];
  f32x16 acc[3];
#pragma unroll
  for (int c = 0; c < 3; ++c)
#pragma unroll
    for (int i = 0; i < 16; ++i) acc[c][i] = 0.f;
  f32x2 rs2 = {0.f, 0.f}, sq2 = {0.f, 0.f};
  f32x4 cs = {0, 0, 0, 0};
  float mean_r = 0.f, rstd = 0.f;
  s16x8 SELF;
  {
    const short v = ((m == 0 && !(g & 1)) || (m == 1 && (g & 1))) ? (short)0x3f80 : (short)0;
#pragma unroll
    for (int i = 0; i < 8; ++i) SELF[i] = v;
  }
  asm volatile("" : "+v"(SELF));

  constexpr int xslot[5] = {0, 2, 4, 6, 0};
  constexpr int wslot[15] = {1, 3, 5, 7, 1, 2, 3, 4, 5, 6, 7, 0, 1, 2, 3};

#define P1_READI(t, i)                                                                         \
  {                                                                                            \
    if ((t) < 5) xf[4 * ((t) % 5) + i] = *(const LAS s16x8*)(L + xslot[(t) % 5] * SLOT + xoff[i]); \
    wf[(t) & 1][i] = trfrag(L + (sb + woff[i]), 512);                                          \
  }
  auto xstat = [&](s16x8 f) {
    u32x4 v = __builtin_bit_cast(u32x4, f);
#pragma unroll
    for (int z = 0; z < 4; ++z) {
      f32x2 a = {__builtin_bit_cast(float, v[z] << 16), __builtin_bit_cast(float, v[z] & 0xffff0000u)};
      rs2 += a;
      sq2 = __builtin_elementwise_fma(a, a, sq2);
    }
  };
#define P1_MMAI(t, i)                                                                          \
  {                                                                                            \
    constexpr int c = (t) / 5, tau = (t) % 5;                                                  \
    acc[c] = mfma32(wf[(t) & 1][i], xf[4 * tau + i], acc[c]);                                  \
    if (i < 2) cs = mfma16(wf[(t) & 1][i], SELF, cs);                                          \
    if (c == 0 && (i & 1) == 0) xstat(xf[4 * tau + i]);                                        \
  }
#define P1_STEP(t, H0, H1, H2, H3)                                                             \
  {                                                                                            \
    int sb = wslot[((t) + 1) < 15 ? (t) + 1 : 14] * SLOT; asm volatile("" : "+s"(sb));         \
    P1_MMAI(t, 0); if ((t) < 14) P1_READI((t) + 1, 0); H0; SCHED();                            \
    P1_MMAI(t, 1); if ((t) < 14) P1_READI((t) + 1, 1); H1; SCHED();                            \
    P1_MMAI(t, 2); if ((t) < 14) P1_READI((t) + 1, 2); H2; SCHED();                            \
    P1_MMAI(t, 3); if ((t) < 14) P1_READI((t) + 1, 3); H3;                                     \
    if ((t) < 5) asm volatile("" : "+v"(rs2), "+v"(sq2));                                  \
    SCHED();                                                                                   \
  }
  f32x4 csk[1];
  auto epi_send = [&](int c) {
    *(LAS f32x4*)(L + P1_RED + (w * 2 + 0) * 1024 + l16) = f32x4{acc[c][8], acc[c][9], acc[c][10], acc[c][11]};
    *(LAS f32x4*)(L + P1_RED + (w * 2 + 1) * 1024 + l16) = f32x4{acc[c][12], acc[c][13], acc[c][14], acc[c][15]};
    if (m < 2) *(LAS f32x4*)(L + P1_SCR + 512 + ((2 * kh + mh) * 192 + 64 * c + 32 * pr + ((16 * m + 4 * g) ^ (16 * kh))) * 4) = csk[0];
    if (c == 0) {
      const float s1 = xsum32(rs2.x + rs2.y), s2 = xsum32(sq2.x + sq2.y);
      if (hh == 0) *(LAS f32x2*)(L + P1_SCR + 3584 + ((2 * kh + pr) * 64 + 32 * mh + m32) * 8) = f32x2{s1, s2};
    }
  };
#define CS_SNAP() { csk[0] = cs; cs = f32x4{0, 0, 0, 0}; }

  f32x4 e_oth[2], e_c[2][4];
  u32x2 e_bb[2];
  f32x2 e_s[4];
  f32x4 pre[2];
  u32x4 ho;
  auto epi_load = [&](int c) {
    const LAS float* csp = (const LAS float*)(L + P1_SCR + 512);
    const LAS unsigned short* bp = (const LAS unsigned short*)(L + P1_SCR);
#pragma unroll
    for (int a = 0; a < 2; ++a) {
      const int n = 64 * c + 32 * pr + 16 * kh + 8 * a + 4 * hh;
      e_oth[a] = *(const LAS f32x4*)(L + P1_RED + ((w ^ 4) * 2 + a) * 1024 + l16);
#pragma unroll
      for (int k = 0; k < 4; ++k) e_c[a][k] = *(const LAS f32x4*)(csp + k * 192 + n);
      e_bb[a] = *(const LAS u32x2*)(bp + n);
    }
    if (c == 0) {
#pragma unroll
      for (int k = 0; k < 4; ++k) e_s[k] = *(const LAS f32x2*)(L + P1_SCR + 3584 + (k * 64 + 32 * mh + m32) * 8);
    }
  };
  auto epi_prep = [&](int c) {
    if (c == 0) {
      const f32x2 st = (e_s[0] + e_s[1]) + (e_s[2] + e_s[3]);
      float mean = st.x * (1.0f / 640.0f);
      float var = __builtin_fmaf(-mean, mean, st.y * (1.0f / 640.0f));
      var = var < 0.f ? 0.f : var;
      rstd = __builtin_amdgcn_rsqf(var + 1e-5f);
      mean_r = -mean * rstd;
    }
#pragma unroll
    for (int a = 0; a < 2; ++a) {
      f32x4 csum = (e_c[a][0] + e_c[a][1]) + (e_c[a][2] + e_c[a][3]);
      f32x4 bv = {__builtin_bit_cast(float, e_bb[a].x << 16), __builtin_bit_cast(float, e_bb[a].x & 0xffff0000u),
                  __builtin_bit_cast(float, e_bb[a].y << 16), __builtin_bit_cast(float, e_bb[a].y & 0xffff0000u)};
      f32x4 mr4 = {mean_r, mean_r, mean_r, mean_r}, rs4 = {rstd, rstd, rstd, rstd};
      f32x4 t = __builtin_elementwise_fma(mr4, csum, bv);
      f32x4 own = {acc[c][4 * a], acc[c][4 * a + 1], acc[c][4 * a + 2], acc[c][4 * a + 3]};
      pre[a] = __builtin_elementwise_fma(own + e_oth[a], rs4, t);
    }
  };
  auto epi_pair = [&](int k) {
    const int a = k >> 1;
    ho[k] = (k & 1) ? gelu2(f32x2{pre[a][2], pre[a][3]}) : gelu2(f32x2{pre[a][0], pre[a][1]});
  };
  const unsigned hso = (unsigned)((m32 >> 4) * 48 * 1024 + ((2 * kh + hh) * 16 + (m32 & 15)) * 16);
  auto epi_store = [&](int c) {
    const int ks = 6 * cc + 2 * c + pr;
    const char* dst = hg + (size_t)((4 * rb + 2 * mh) * 48 + ks) * 1024;
    asm volatile("global_store_dwordx4 %0, %1, %2 sc1\n\ts_nop 1" :: "v"(hso), "v"(ho), "s"(dst) : "memory");
  };
  auto publish = [&](int nchunks) {
    if (tid == 0) {
      u64 v = tag + EPOCH_STEP + (u64)nchunks;
      asm volatile("global_store_dwordx2 %0, %1, %2 sc1\n\ts_nop 1" :: "v"(0), "v"(v), "s"(ws + WS_FLAGS + rb * 256 + cc * 8) : "memory");
    }
  };

  STAMP(1);
  WAITBAR(13); STAMP(2);
  asm volatile("" : "+v"(tag));
  { int sb = wslot[0] * SLOT; P1_READI(0, 0); P1_READI(0, 1); P1_READI(0, 2); P1_READI(0, 3); }
  WAITBAR(8); STAMP(3);
  P1_STEP(0, {issueX1(4, 0, 0);}, {issueX1(4, 0, 1);}, {issueW1(4, 1, 0);}, {issueW1(4, 1, 1);});
  WAITBAR(8); STAMP(4);
  P1_STEP(1, {issueW1(5, 2, 0);}, {issueW1(5, 2, 1);}, {issueW1(6, 3, 0);}, {issueW1(6, 3, 1);});
  WAITBAR(8); STAMP(5);
  P1_STEP(2, {issueW1(7, 4, 0);}, {issueW1(7, 4, 1);}, {issueW1(8, 5, 0);}, {issueW1(8, 5, 1);});
  WAITBAR(8); STAMP(6);
  P1_STEP(3, {issueW1(9, 6, 0);}, {issueW1(9, 6, 1);}, {issueW1(10, 7, 0);}, {issueW1(10, 7, 1);});
  WAITBAR(10); STAMP(7);
  P1_STEP(4, {issueW1(11, 0, 0);}, {issueW1(11, 0, 1);}, {issueW1(12, 1, 0);}, {issueW1(12, 1, 1);});
  CS_SNAP();
  WAITBAR(12); STAMP(8);
  P1_STEP(5, {epi_send(0);}, {issueW1(13, 2, 0);}, {issueW1(13, 2, 1);}, {NOP;});
  WAITBAR(12); STAMP(9);
  P1_STEP(6, {epi_load(0);}, {issueW1(14, 3, 0);}, {issueW1(14, 3, 1);}, {epi_prep(0);});
  WAITBAR(12); STAMP(10);
  P1_STEP(7, {issueV1(0, 0, 4, 0);}, {epi_pair(0);}, {issueV1(0, 0, 4, 1);}, {epi_pair(1);});
  WAITBAR(12); STAMP(11);
  P1_STEP(8, {issueV1(1, 0, 5, 0);}, {epi_pair(2);}, {issueV1(1, 0, 5, 1);}, {epi_pair(3); epi_store(0);});
  WAITBAR(13); STAMP(12);
  P1_STEP(9, {issueV1(2, 0, 6, 0);}, {NOP;}, {issueV1(2, 0, 6, 1);}, {NOP;});
  CS_SNAP();
  WAITBAR(13); STAMP(13);
  P1_STEP(10, {epi_send(1);}, {issueV1(3, 0, 7, 0);}, {issueV1(3, 0, 7, 1);}, {NOP;});
  WAITBAR(4); STAMP(14);
  P1_STEP(11, {publish(1); epi_load(1);}, {issueV1(0, 1, 0, 0);}, {issueV1(0, 1, 0, 1);}, {epi_prep(1);});
  WAITBAR(13); STAMP(15);
  P1_STEP(12, {issueV1(1, 1, 1, 0);}, {epi_pair(0);}, {issueV1(1, 1, 1, 1);}, {epi_pair(1);});
  WAITBAR(13); STAMP(16);
  P1_STEP(13, {issueV1(2, 1, 2, 0);}, {epi_pair(2);}, {issueV1(2, 1, 2, 1);}, {epi_pair(3); epi_store(1);});
  WAITBAR(16); STAMP(17);
  P1_STEP(14, {issueV1(3, 1, 3, 0);}, {NOP;}, {issueV1(3, 1, 3, 1);}, {NOP;});
  CS_SNAP();
  epi_send(2);
  WAITBAR(2); STAMP(18);
  publish(2);
  epi_load(2); epi_prep(2); epi_pair(0); epi_pair(1); epi_pair(2); epi_pair(3); epi_store(2);
  WAITBAR(0); STAMP(19);
  publish(3);

  const int rt0 = 6 * rb2 + 3 * mh2;
  const int rbA = (6 * rb2) >> 2, rbB = ((6 * rb2 + 5) >> 2) > 31 ? 31 : ((6 * rb2 + 5) >> 2);
  auto poll = [&](int nchunks) {
    if (w == 0) {
      const unsigned foff = (unsigned)(((lane & 8) ? rbB : rbA) * 256 + (lane & 7) * 8);
      const u64 base = tag + EPOCH_STEP;
      for (unsigned spins = 0; spins < SPIN_CAP; ++spins) {
        u64 f;
        asm volatile("global_load_dwordx2 %0, %1, %2 sc1\n\ts_waitcnt vmcnt(0)" : "=&v"(f) : "v"(foff), "s"(ws + WS_FLAGS) : "memory");
        const u64 d = f - base;
        const bool ok = d >= (u64)nchunks && d <= 3ull;
        if (__all(ok)) break;
        __builtin_amdgcn_s_sleep(1);
      }
    }
  };
  if (!has2) {
    if (blockIdx.x == 255 && w == 0) {
      const u64 base = tag + EPOCH_STEP;
      for (unsigned spins = 0; spins < SPIN_CAP; ++spins) {
        bool ok = true;
#pragma unroll
        for (int k = 0; k < 4; ++k) {
          u64 f;
          const unsigned foff = (unsigned)((lane >> 1) * 256 + ((lane & 1) * 4 + k) * 8);
          asm volatile("global_load_dwordx2 %0, %1, %2 sc1\n\ts_waitcnt vmcnt(0)" : "=&v"(f) : "v"(foff), "s"(ws + WS_FLAGS) : "memory");
          const u64 d = f - base;
          ok = ok && d >= 1ull && d <= 3ull;
        }
        if (__all(ok)) break;
        __builtin_amdgcn_s_sleep(4);
      }
      if (lane == 0) asm volatile("global_store_dwordx2 %0, %1, %2 sc1\n\ts_nop 1" :: "v"(0), "v"(base), "s"(ws + WS_EPOCH) : "memory");
    }
    STAMP_OUT(ws + (64u << 20), 19);
    return;
  }

  u32x4 hf[3][4][3];
  auto issueH = [&](int s) {
#pragma unroll
    for (int k4 = 0; k4 < 4; ++k4)
#pragma unroll
      for (int r = 0; r < 3; ++r) {
        int rt = rt0 + r; rt = rt > 127 ? 127 : rt;
        const int ks = 12 * kq + 6 * (k4 >> 1) + 2 * s + (k4 & 1);
        const char* sb = hg + (size_t)(rt * 48 + ks) * 1024;
        asm volatile("global_load_dwordx4 %0, %1, %2 sc1" : "=v"(hf[s][k4][r]) : "v"(l16), "s"(sb) : "memory");
      }
  };
  int voff[4];
  {
    const int r = 4 * (g & 1) + q;
#pragma unroll
    for (int t = 0; t < 4; ++t)
      voff[t] = kq * SLOT + (g >> 1) * 2048 + (8 * r + ((2 * t + (p >> 1)) ^ ((r >> 1) << 1))) * 16 + (p & 1) * 8;
  }
  f32x4 acc2[3][4];
#pragma unroll
  for (int r = 0; r < 3; ++r)
#pragma unroll
    for (int t = 0; t < 4; ++t) acc2[r][t] = f32x4{0, 0, 0, 0};
#define P2_COMPUTE(s, slot0)                                                                   \
  {                                                                                            \
    LAS char* wb = L + (slot0) * SLOT;                                                         \
    _Pragma("unroll") for (int k4 = 0; k4 < 4; ++k4) {                                         \
      _Pragma("unroll") for (int t = 0; t < 4; ++t) {                                          \
        s16x8 vf = trfrag(wb + voff[t] + k4 * 4096, 1024);                                     \
        _Pragma("unroll") for (int r = 0; r < 3; ++r)                                          \
          acc2[r][t] = mfma16(vf, __builtin_bit_cast(s16x8, hf[s][k4][r]), acc2[r][t]);        \
      }                                                                                        \
    }                                                                                          \
  }
#define PIN_H(s) _Pragma("unroll") for (int k4 = 0; k4 < 4; ++k4) _Pragma("unroll") for (int r = 0; r < 3; ++r) asm volatile("" : "+v"(hf[s][k4][r]));

  poll(2);
  BAR(); STAMP(20);
  issueH(0); issueH(1);
  WAIT_V(12); PIN_H(0); STAMP(21);
  P2_COMPUTE(0, 4);
  STAMP(22);
  poll(3);
  WAITBAR(0); PIN_H(1); STAMP(23);
#pragma unroll
  for (int k = 0; k < 4; ++k) { issueV1(k, 2, 4 + k, 0); issueV1(k, 2, 4 + k, 1); }
  issueH(2);
  P2_COMPUTE(1, 0);
  WAITBAR(0); PIN_H(2); STAMP(24);
  u32x2 b2v = *(const u32x2*)(b2g + 64 * line + 16 * kq + 4 * g);
  {
    LAS char* wb = L + 4 * SLOT;
#pragma unroll
    for (int t = 0; t < 4; ++t) {
#pragma unroll
      for (int k4 = 0; k4 < 4; ++k4) {
        s16x8 vf = trfrag(wb + voff[t] + k4 * 4096, 1024);
#pragma unroll
        for (int r = 0; r < 3; ++r) acc2[r][t] = mfma16(vf, __builtin_bit_cast(s16x8, hf[2][k4][r]), acc2[r][t]);
      }
      if (t != kq) {
        const int grp = mh2 * 4 + t, j = (kq - t - 1) & 3;
        LAS char* rp = L + (grp < 7 ? grp * 9216 : 8 * SLOT) + j * 3072 + l16;
#pragma unroll
        for (int r = 0; r < 3; ++r) *(LAS f32x4*)(rp + r * 1024) = acc2[r][t];
      }
    }
  }
  WAIT_L0(); BAR(); STAMP(25);
  {
    const int d0 = 64 * line + 16 * kq + 4 * g;
    const float bv[4] = {__builtin_bit_cast(float, b2v.x << 16), __builtin_bit_cast(float, b2v.x & 0xffff0000u),
                         __builtin_bit_cast(float, b2v.y << 16), __builtin_bit_cast(float, b2v.y & 0xffff0000u)};
    const int grp = mh2 * 4 + kq;
    LAS char* rp = L + (grp < 7 ? grp * 9216 : 8 * SLOT) + l16;
#pragma unroll
    for (int r = 0; r < 3; ++r) {
      f32x4 s = kq == 0 ? acc2[r][0] : kq == 1 ? acc2[r][1] : kq == 2 ? acc2[r][2] : acc2[r][3];
#pragma unroll
      for (int j = 0; j < 3; ++j) s += *(const LAS f32x4*)(rp + j * 3072 + r * 1024);
      const int rt = rt0 + r;
      if (rt < 128) {
        u32x2 ov = {pack2(s[0] + bv[0], s[1] + bv[1]), pack2(s[2] + bv[2], s[3] + bv[3])};
        *(u32x2*)(outg + (size_t)(16 * rt + m) * 640 + d0) = ov;
      }
    }
  }
  STAMP(26);
  STAMP_OUT(ws + (64u << 20), 26);
}

extern "C" void kernel_launch(void* const* d_in, const int* in_sizes, int n_in, void* d_out, int out_size, void* d_ws,
                              size_t ws_size, hipStream_t stream) {
  (void)hipFuncSetAttribute((const void*)mixer_fused, hipFuncAttributeMaxDynamicSharedMemorySize, LDS_BYTES);
  mixer_fused<<<256, 512, LDS_BYTES, stream>>>((const __hip_bfloat16*)d_in[0], (const __hip_bfloat16*)d_in[1], (const __hip_bfloat16*)d_in[2],
                                               (const __hip_bfloat16*)d_in[3], (const __hip_bfloat16*)d_in[4], (__hip_bfloat16*)d_out,
                                               (char*)d_ws);
}
```

```cpp
#include <hip/hip_runtime.h>
#include <hip/hip_bf16.h>
#include <stdint.h>
#define PFC 1
#define PFC_WAVE 0
#define PFC_BYTES 31744u

typedef __bf16 bf16x2 __attribute__((ext_vector_type(2)));
typedef __bf16 bf16x8 __attribute__((ext_vector_type(8)));
typedef short s16x4 __attribute__((ext_vector_type(4)));
typedef short s16x8 __attribute__((ext_vector_type(8)));
typedef float f32x2 __attribute__((ext_vector_type(2)));
typedef float f32x4 __attribute__((ext_vector_type(4)));
typedef float f32x16 __attribute__((ext_vector_type(16)));
typedef unsigned u32x2 __attribute__((ext_vector_type(2)));
typedef unsigned u32x4 __attribute__((ext_vector_type(4)));
typedef unsigned long long u64;
#define LAS __attribute__((address_space(3)))
#define GAS __attribute__((address_space(1)))
#define DI __device__ __forceinline__

#define WAIT_V(n) asm volatile("s_waitcnt vmcnt(" #n ")" ::: "memory")
#define WAIT_L0() asm volatile("s_waitcnt lgkmcnt(0)" ::: "memory")
#define BAR() asm volatile("s_barrier" ::: "memory")
#define WAITBAR(n) asm volatile("s_waitcnt vmcnt(" #n ")\n\ts_barrier" ::: "memory")
#define LGKM0() do { __builtin_amdgcn_sched_barrier(0); __builtin_amdgcn_s_waitcnt(0xC07F); __builtin_amdgcn_sched_barrier(0); } while (0)
#define WAITLBAR() do { LGKM0(); asm volatile("s_barrier" ::: "memory"); __builtin_amdgcn_sched_barrier(0); } while (0)
#define SCHED() __builtin_amdgcn_sched_barrier(0)
#define NOP (void)0

DI u32x4 make_srd(const void* p, unsigned bytes) {
  const uint64_t a = (uint64_t)p;
  u32x4 r;
  r[0] = __builtin_amdgcn_readfirstlane((unsigned)a);
  r[1] = __builtin_amdgcn_readfirstlane((unsigned)(a >> 32) & 0xffffu);
  r[2] = __builtin_amdgcn_readfirstlane(bytes);
  r[3] = 0x00020000u;
  return r;
}
#define DMA(voff, srd, SOFF, LOFF) do { unsigned tmp_; \
  asm volatile("s_add_u32 m0, %2, %5\n\ts_mov_b32 %0, %4\n\tbuffer_load_dwordx4 %1, %3, %0 offen lds" \
               : "=&s"(tmp_) : "v"(voff), "s"(ldsw), "s"(srd), "i"(SOFF), "i"(LOFF) : "memory", "scc"); } while (0)
#define DMAR(voff, srd, sbase, SOFF, LOFF) do { unsigned tmp_; \
  asm volatile("s_add_u32 m0, %2, %6\n\ts_add_u32 %0, %4, %5\n\tbuffer_load_dwordx4 %1, %3, %0 offen lds" \
               : "=&s"(tmp_) : "v"(voff), "s"(ldsw), "s"(srd), "s"(sbase), "i"(SOFF), "i"(LOFF) : "memory", "scc"); } while (0)

DI f32x4 mfma16(s16x8 a, s16x8 b, f32x4 c) {
  return __builtin_amdgcn_mfma_f32_16x16x32_bf16(__builtin_bit_cast(bf16x8, a), __builtin_bit_cast(bf16x8, b), c, 0, 0, 0);
}
DI f32x16 mfma32(s16x8 a, s16x8 b, f32x16 c) {
  return __builtin_amdgcn_mfma_f32_32x32x16_bf16(__builtin_bit_cast(bf16x8, a), __builtin_bit_cast(bf16x8, b), c, 0, 0, 0);
}
DI s16x4 trread(LAS char* p) { return __builtin_amdgcn_ds_read_tr16_b64_v4i16((LAS s16x4*)p); }
DI s16x8 trfrag(LAS char* p, int second) {
  s16x4 lo = trread(p), hi = trread(p + second);
  return __builtin_shufflevector(lo, hi, 0, 1, 2, 3, 4, 5, 6, 7);
}
DI unsigned pack2(float lo, float hi) {
  f32x2 v = {lo, hi};
  return __builtin_bit_cast(unsigned, __builtin_convertvector(v, bf16x2));
}
DI unsigned gelu2(f32x2 v, f32x2 k1, f32x2 k2) {
  f32x2 u = v * v;
  f32x2 t = __builtin_elementwise_fma(u, k2, k1);
  f32x2 z = v * t;
  f32x2 e = {__builtin_amdgcn_exp2f(z.x), __builtin_amdgcn_exp2f(z.y)};
  f32x2 d = e + f32x2{1.0f, 1.0f};
  f32x2 r = {__builtin_amdgcn_rcpf(d.x), __builtin_amdgcn_rcpf(d.y)};
  f32x2 o = v * r;
  return __builtin_bit_cast(unsigned, __builtin_convertvector(o, bf16x2));
}
DI float xsum32(float v) {
  unsigned a = __builtin_bit_cast(unsigned, v);
  auto r = __builtin_amdgcn_permlane32_swap(a, a, false, false);
  unsigned r0 = r[0], r1 = r[1];
  return __builtin_bit_cast(float, r0) + __builtin_bit_cast(float, r1);
}

constexpr int UNIT = 8192;
constexpr int SCR = 19 * UNIT;
constexpr int S_B1 = SCR, S_B1F = SCR + 512, S_CS = SCR + 1280, S_ST = SCR + 2816, S_DUM = SCR + 4096;
constexpr int LDS_BYTES = 20 * UNIT;
constexpr size_t WS_EPOCH = 0;
constexpr size_t WS_FLAGS = 4096;
constexpr size_t WS_H = 1u << 20;
constexpr u64 EPOCH_STEP = 0x9E3779B97F4A7C15ull;
constexpr unsigned SPIN_CAP = 1u << 16;

__global__ __launch_bounds__(512, 2) void mixer_fused(const __hip_bfloat16* __restrict__ xg, const __hip_bfloat16* __restrict__ w1g,
                                                      const __hip_bfloat16* __restrict__ b1g, const __hip_bfloat16* __restrict__ w2g,
                                                      const __hip_bfloat16* __restrict__ b2g, __hip_bfloat16* __restrict__ outg,
                                                      char* __restrict__ ws) {
  extern __shared__ __attribute__((aligned(16))) char smem_[];
  LAS char* L = (LAS char*)smem_;
  const int tid = threadIdx.x, lane = tid & 63;
  const int w = __builtin_amdgcn_readfirstlane(tid >> 6);
  unsigned pf_code = 0;
  if (w == PFC_WAVE) {
    unsigned long long pc;
    asm volatile("s_getpc_b64 %0" : "=s"(pc));
    const char* cb = (const char*)(pc & ~127ull);
    const unsigned o3 = 24576u + lane * 128u < PFC_BYTES ? 24576u + lane * 128u : PFC_BYTES - 128u;
    asm volatile("global_load_dword %0, %1, %3\n\tglobal_load_dword %0, %1, %4\n\tglobal_load_dword %0, %1, %5\n\tglobal_load_dword %0, %2, %3"
                 : "=&v"(pf_code) : "v"(lane * 128), "v"(o3), "s"(cb), "s"(cb + 8192), "s"(cb + 16384) : "memory");
  }
  const int xcd = blockIdx.x & 7, idx = blockIdx.x >> 3;
  const int rb = 8 * (xcd >> 1) + (idx >> 2), cc = 4 * (xcd & 1) + (idx & 3);
  const int rbg = xcd >> 1;
  const int rbase = rbg * 6 - (rbg > 2 ? 1 : 0);
  const int rl = idx / 5;
  const bool has2 = rl < (rbg < 2 ? 6 : 5);
  const int rb2 = has2 ? rbase + rl : 0, line = 5 * (xcd & 1) + idx % 5;

  if (__builtin_expect(w >= 4, 1)) {
    const int l = w - 4;
    const unsigned ldsw = (unsigned)(uintptr_t)L + l * 1024;
    const unsigned xo = (unsigned)(64 * rb + 8 * l) * 1280u, wo = (unsigned)(8 * l) * 3072u + (unsigned)cc * 384u;
    const u32x4 sx = make_srd((const char*)xg + xo, 2048u * 1280u - xo);
    const u32x4 sw = make_srd((const char*)w1g + wo, 640u * 3072u - wo);
    const unsigned xv = (lane >> 3) * 1280 + (((lane & 7) ^ ((4 * l + (lane >> 4)) & 7)) << 4);
    const unsigned wv = (lane >> 3) * 3072 + (((lane & 7) ^ (((lane >> 4) & 1) << 2)) << 4);
    const int rot = (((xcd >> 1) * 5) / 2 + (xcd & 1) * 5) % 10;
    unsigned kk[10];
#pragma unroll
    for (int i = 0; i < 10; ++i) { kk[i] = __builtin_amdgcn_readfirstlane((unsigned)((i + rot) % 10)); }
#define LD_X(h, j, unit) do { const unsigned kb_ = kk[5 * (h) + (j)] * 128u; DMAR(xv, sx, kb_, 0, (unit) * UNIT); DMAR(xv, sx, kb_, 32 * 1280, (unit) * UNIT + 4096); } while (0)
#define LD_W(h, c, j, unit) do { const unsigned kb_ = kk[5 * (h) + (j)] * 196608u; DMAR(wv, sw, kb_, (c) * 128, (unit) * UNIT); DMAR(wv, sw, kb_, 32 * 3072 + (c) * 128, (unit) * UNIT + 4096); } while (0)

    u64 tag;
    LD_X(0, 0, 13);
    LD_W(0, 0, 0, 0);
    LD_X(0, 1, 12);
    LD_X(0, 2, 11);
    LD_W(0, 0, 1, 1);
    { const u32x4 sb1 = make_srd((const char*)b1g + cc * 384 + l * 96, 96); if (lane < 6) { const unsigned ldsw = (unsigned)(uintptr_t)L + l * 96; DMA(lane * 16, sb1, 0, S_B1); } }
    asm volatile("global_load_dwordx2 %0, %1, %2 sc1" : "=v"(tag) : "v"(0), "s"(ws + WS_EPOCH) : "memory");
    LD_W(0, 0, 2, 2);
    LD_X(0, 3, 7);
    LD_X(0, 4, 6);
    LD_W(0, 0, 3, 3);
    LD_W(0, 0, 4, 4);
    LD_X(1, 0, 18);
    LD_X(1, 1, 17);
    LD_W(1, 0, 0, 5);
    LD_W(1, 0, 1, 8);
    LD_X(1, 2, 16);
    LD_X(1, 3, 15);
    LD_W(1, 0, 2, 9);
    LD_W(1, 0, 3, 10);
    LD_X(1, 4, 14);

    const unsigned vo = (unsigned)(8 * l) * 1280u + (unsigned)line * 128u;
    const u32x4 sv = make_srd((const char*)w2g + vo, 1536u * 1280u - vo);
    const unsigned vv = has2 ? (lane >> 3) * 1280 + (((lane & 7) ^ ((lane >> 4) << 1)) << 4) : 0u;
#define LD_V(ks0, ks1, unit) do { DMA(vv, sv, (ks0) * 32 * 1280, (unit) * UNIT); DMA(vv, sv, (ks1) * 32 * 1280, (unit) * UNIT + 4096); } while (0)

    WAITBAR(36);

    asm volatile("" : "+v"(tag));
    WAITBAR(26);

    LD_W(1, 0, 4, 0);
    LD_W(0, 1, 0, 13);
    WAITBAR(22);

    LD_W(0, 1, 1, 1);
    LD_W(0, 1, 2, 2);
    LD_W(0, 1, 3, 11);
    LD_W(0, 1, 4, 12);
    WAITBAR(22);

    LD_W(1, 1, 0, 3);
    LD_W(1, 1, 1, 4);
    LD_W(1, 1, 2, 6);
    LD_W(1, 1, 3, 7);
    WAITBAR(22);

    LD_W(1, 1, 4, 5);
    LD_W(0, 2, 0, 8);
    WAITBAR(20);

    LD_W(0, 2, 1, 9);
    LD_W(0, 2, 2, 10);
    WAITBAR(20);

    LD_W(0, 2, 3, 0);
    LD_W(0, 2, 4, 13);
    WAITBAR(20);

    LD_W(1, 2, 0, 1);
    LD_W(1, 2, 1, 2);
    WAITBAR(20);

    LD_W(1, 2, 2, 11);
    LD_W(1, 2, 3, 12);
    WAITBAR(20);

    LD_W(1, 2, 4, 3);
    LD_V(0, 24, 4);
    WAITBAR(20);

    LD_V(1, 25, 6);
    LD_V(6, 30, 7);
    WAITBAR(20);

    LD_V(7, 31, 5);
    LD_V(12, 36, 8);
    WAITBAR(20);

    LD_V(13, 37, 9);
    LD_V(18, 42, 10);
    WAITBAR(20);

    LD_V(19, 43, 0);
    LD_V(2, 26, 13);
    WAITBAR(20);

    LD_V(3, 27, 1);
    LD_V(8, 32, 2);
    WAITBAR(22);

    LD_V(9, 33, 11);
    LD_V(14, 38, 12);
    LD_V(15, 39, 15);
    BAR();

    LD_V(20, 44, 3);
    LD_V(21, 45, 14);
    BAR();

    if (!has2) {
      if (blockIdx.x == 255 && l == 0) {
        const u64 base = tag + EPOCH_STEP;
        for (unsigned spins = 0; spins < SPIN_CAP; ++spins) {
          bool ok = true;
          u64 f[16];
#pragma unroll
          for (int k = 0; k < 16; ++k)
            asm volatile("global_load_dwordx2 %0, %1, %2 sc1" : "=v"(f[k]) : "v"((unsigned)(WS_FLAGS + (k * 64 + lane) * 8)), "s"(ws) : "memory");
          asm volatile("s_waitcnt vmcnt(0)" : "+v"(f[0]), "+v"(f[1]), "+v"(f[2]), "+v"(f[3]), "+v"(f[4]), "+v"(f[5]), "+v"(f[6]), "+v"(f[7]),
                       "+v"(f[8]), "+v"(f[9]), "+v"(f[10]), "+v"(f[11]), "+v"(f[12]), "+v"(f[13]), "+v"(f[14]), "+v"(f[15]) :: "memory");
#pragma unroll
          for (int k = 0; k < 16; ++k) { const u64 d = f[k] - base; ok = ok && d >= 1ull && d <= 3ull; }
          if (__all(ok)) break;
          __builtin_amdgcn_s_sleep(4);
        }
        if (lane == 0) asm volatile("global_store_dwordx2 %0, %1, %2 sc1\n\ts_nop 1" :: "v"(0), "v"(base), "s"(ws + WS_EPOCH) : "memory");
      }
      WAIT_V(0);

      return;
    }
    WAITBAR(30);

    WAITBAR(26);

    LD_V(4, 28, 4);
    WAITBAR(24);

    LD_V(5, 29, 6);
    LD_V(10, 34, 7);
    WAITBAR(24);

    LD_V(11, 35, 5);
    LD_V(16, 40, 8);
    WAITBAR(24);

    LD_V(17, 41, 9);
    LD_V(22, 46, 10);
    WAITBAR(24);

    LD_V(23, 47, 0);
    WAITBAR(22);

    WAITBAR(18);

    WAITBAR(14);

    WAITBAR(10);

    WAITBAR(6);

    WAITBAR(2);

    WAITBAR(0);

    BAR();

    BAR();

    return;
  }

  unsigned pf_sink = 0;
#define PFSTAG 1
  unsigned pfo1, pfo2;
  {
    const int i1 = w * 40 + (lane < 40 ? lane : 39), l8 = i1 & 7;
    pfo1 = (unsigned)((20 * idx + (i1 >> 3)) * 3072 + (xcd & 1) * 1536 + ((l8 >> 1) * 3 + 1 + (l8 & 1)) * 128);
    const int i0 = w * 60 + (lane < 60 ? lane : 59);
    pfo2 = (unsigned)((48 * idx + i0 / 5) * 1280 + (xcd & 1) * 640 + (i0 % 5) * 128);
  }
#define PF_W1() asm volatile("global_load_dword %0, %1, %2" : "+v"(pf_sink) : "v"(pfo1), "s"(w1g) : "memory")
#define PF_W2() asm volatile("global_load_dword %0, %1, %2" : "+v"(pf_sink) : "v"(pfo2), "s"(w2g) : "memory")
  const int m = lane & 15, g = lane >> 4, q = (lane & 15) >> 2, p = lane & 3;
  const int hh = lane >> 5, m32 = lane & 31;
  const int mh = w & 1, pr = w >> 1;
  const char* b2p = (const char*)b2g; char* outp = (char*)outg;
  asm volatile("" : "+s"(b2p), "+s"(outp), "+s"(ws));
  char* hg = ws + WS_H;
  int xoff[4], xoffH[4], woffL[4], woffH[4];
#pragma unroll
  for (int i = 0; i < 4; ++i) {
    const int ks = (i + 2 * mh + pr) & 3;
    const int row = 32 * mh + m32;
    xoff[i] = (8 * row + ((2 * ks + hh) ^ ((row >> 1) & 7))) * 16;
    const int c = (4 * pr + 2 * (g & 1) + (p >> 1)) ^ (((q >> 1) & 1) << 2);
    woffL[i] = (2 * ks + hh) * 1024 + (8 * q + c) * 16 + (p & 1) * 8;
    woffH[i] = woffL[i] + 65536; xoffH[i] = xoff[i] + 11 * UNIT;
    asm volatile("" : "+v"(xoff[i]), "+v"(xoffH[i]), "+v"(woffL[i]), "+v"(woffH[i]));
  }
  s16x8 xf[20], xh[2][4];
  s16x8 wf[2][4];
  f32x16 acc0, acc1, acc2_, sq16;
#pragma unroll
  for (int i = 0; i < 16; ++i) { acc0[i] = 0.f; acc1[i] = 0.f; acc2_[i] = 0.f; sq16[i] = 0.f; }
  f32x4 rs = {0, 0, 0, 0}, cs0 = {0, 0, 0, 0}, cs1 = {0, 0, 0, 0}, cs2 = {0, 0, 0, 0};
  float mean_r = 0.f, rstd = 0.f;
  s16x8 SEL;
  {
    const short v = ((m == 0 && !(g & 1)) || (m == 4 && (g & 1))) ? (short)0x3f80 : (short)0;
#pragma unroll
    for (int i = 0; i < 8; ++i) SEL[i] = v;
  }
  asm volatile("" : "+v"(SEL));
  f32x2 GK1, GK2;
  { constexpr float C1 = -2.0f * 0.7978845608028654f * 1.4426950408889634f; GK1 = f32x2{C1, C1}; GK2 = f32x2{C1 * 0.044715f, C1 * 0.044715f}; }
  asm volatile("" : "+v"(GK1), "+v"(GK2));
#define RD_X(dst, i, unit) dst = *(const LAS s16x8*)(L + ((unit) < 8 ? xoff[i] : xoffH[i]) + ((unit) < 8 ? (unit) : (unit) - 11) * UNIT)
#define RD_W(buf, i, unit) wf[buf][i] = trfrag(L + ((unit) < 8 ? woffL[i] : woffH[i]) + ((unit) & 7) * UNIT, 512)
  int a_cs = S_CS + (mh * 192 + 32 * pr + 4 * (m & 4) + 4 * g) * 4;
  int a_ec = S_CS + (32 * pr + 4 * hh) * 4;
  int a_eb = S_B1F + (32 * pr + 4 * hh) * 4;
  asm volatile("" : "+v"(a_cs), "+v"(a_ec), "+v"(a_eb));
#define SEND_CS(c, csv) if ((m & 11) == 0) *(LAS f32x4*)(L + a_cs + 256 * (c)) = csv
  auto send_st = [&]() {
    const bool c0 = m32 & 1, c1 = m32 & 2, c3 = m32 & 8, c4 = m32 & 16;
    float q0 = sq16[0];
    float q1 = sq16[1];
    float q2 = sq16[2];
    float q3 = sq16[3];
    float q4 = sq16[4];
    float q5 = sq16[5];
    float q6 = sq16[6];
    float q7 = sq16[7];
    float q8 = sq16[8];
    float q9 = sq16[9];
    float q10 = sq16[10];
    float q11 = sq16[11];
    float q12 = sq16[12];
    float q13 = sq16[13];
    float q14 = sq16[14];
    float q15 = sq16[15];
    asm volatile("" : "+v"(q0), "+v"(q1), "+v"(q2), "+v"(q3), "+v"(q4), "+v"(q5), "+v"(q6), "+v"(q7), "+v"(q8), "+v"(q9), "+v"(q10), "+v"(q11), "+v"(q12), "+v"(q13), "+v"(q14), "+v"(q15));
    const float r0 = c0 ? q1 : q0;
    const float r1 = c0 ? q3 : q2;
    const float r2 = c0 ? q5 : q4;
    const float r3 = c0 ? q7 : q6;
    const float r4 = c0 ? q9 : q8;
    const float r5 = c0 ? q11 : q10;
    const float r6 = c0 ? q13 : q12;
    const float r7 = c0 ? q15 : q14;
    const float t0 = c1 ? r1 : r0;
    const float t1 = c1 ? r3 : r2;
    const float t2 = c1 ? r5 : r4;
    const float t3 = c1 ? r7 : r6;
    const float u0 = c3 ? t1 : t0;
    const float u1 = c3 ? t3 : t2;
    const float d = c4 ? u1 : u0;
    const int real = S_ST + (32 * mh + m32) * 16 + pr * 8;
    *(LAS float*)(L + (hh == 0 ? real : S_DUM + lane * 8)) = rs[0];
    *(LAS float*)(L + (hh == ((m32 >> 2) & 1) ? real + 4 : S_DUM + lane * 8 + 4)) = d;
  };
  auto b1_cvt = [&]() {
    if (lane < 48) *(LAS float*)(L + S_B1F + (w * 48 + lane) * 4) = __builtin_bit_cast(float, (unsigned)*(const LAS unsigned short*)(L + S_B1 + (w * 48 + lane) * 2) << 16);
  };
  f32x4 e_c0[4], e_c1[4], e_b[4], e_s;
  f32x4 pre;
  u32x4 ho[2];
#define CS_ADD(a) asm volatile("v_add_f32 %0, %0, %4\n\tv_add_f32 %1, %1, %5\n\tv_add_f32 %2, %2, %6\n\tv_add_f32 %3, %3, %7" : "+v"(e_c0[a][0]), "+v"(e_c0[a][1]), "+v"(e_c0[a][2]), "+v"(e_c0[a][3]) : "v"(e_c1[a][0]), "v"(e_c1[a][1]), "v"(e_c1[a][2]), "v"(e_c1[a][3]))
#define EPI_LOAD(c, a) do { e_c0[a] = *(const LAS f32x4*)(L + a_ec + 256 * (c) + 32 * (a)); e_c1[a] = *(const LAS f32x4*)(L + a_ec + 768 + 256 * (c) + 32 * (a)); \
                            e_b[a] = *(const LAS f32x4*)(L + a_eb + 256 * (c) + 32 * (a)); } while (0)
  auto epi_load_st = [&]() { e_s = *(const LAS f32x4*)(L + S_ST + (32 * mh + m32) * 16); };
  auto epi_stats = [&]() {
    const float s1 = e_s[0] + e_s[2], s2 = e_s[1] + e_s[3];
    float mean = s1 * (1.0f / 640.0f);
    float var = __builtin_fmaf(-mean, mean, s2 * (1.0f / 640.0f));
    var = var < 0.f ? 0.f : var;
    rstd = __builtin_amdgcn_rsqf(var + 1e-5f);
    mean_r = -mean * rstd;
    asm volatile("" : "+v"(rstd), "+v"(mean_r));
  };
  float ev0, ev1, ez0, ez1;
  const float kC1 = -2.0f * 0.7978845608028654f * 1.4426950408889634f;
  float vC1 = kC1; asm volatile("" : "+v"(vC1));
#define V_ADD(d, a_, b_) asm volatile("v_add_f32 %0, %1, %2" : "=v"(d) : "v"(a_), "v"(b_))
#define V_MUL(d, a_, b_) asm volatile("v_mul_f32 %0, %1, %2" : "=v"(d) : "v"(a_), "v"(b_))
#define V_FMA(d, a_, b_, c_) asm volatile("v_fma_f32 %0, %1, %2, %3" : "=v"(d) : "v"(a_), "v"(b_), "v"(c_))
#define E_S1(accv, a, hf_) asm volatile("v_fma_f32 %0, %2, %4, %6\n\tv_fma_f32 %1, %2, %5, %7\n\tv_fma_f32 %0, %8, %3, %0\n\tv_fma_f32 %1, %9, %3, %1" \
    : "=&v"(ev0), "=&v"(ev1) : "v"(mean_r), "v"(rstd), "v"(e_c0[a][2 * (hf_)]), "v"(e_c0[a][2 * (hf_) + 1]), "v"(e_b[a][2 * (hf_)]), "v"(e_b[a][2 * (hf_) + 1]), \
      "v"(accv[4 * (a) + 2 * (hf_)]), "v"(accv[4 * (a) + 2 * (hf_) + 1]))
#define E_S2() asm volatile("v_mul_f32 %0, %2, %2\n\tv_mul_f32 %1, %3, %3\n\tv_fmamk_f32 %0, %0, 0xbdd2d3e7, %4\n\tv_fmamk_f32 %1, %1, 0xbdd2d3e7, %4\n\tv_mul_f32 %0, %2, %0\n\tv_mul_f32 %1, %3, %1" \
    : "=&v"(ez0), "=&v"(ez1) : "v"(ev0), "v"(ev1), "v"(vC1))
#define E_S3() do { asm volatile("v_exp_f32 %0, %1\n\tv_exp_f32 %2, %3" : "=&v"(ez0), "+v"(ez0), "=&v"(ez1), "+v"(ez1)); } while (0)
#define E_S3X() asm volatile("v_exp_f32 %0, %0\n\tv_exp_f32 %1, %1\n\tv_add_f32 %0, 1.0, %0\n\tv_add_f32 %1, 1.0, %1" : "+v"(ez0), "+v"(ez1))
#define E_S4(a, hf_) do { unsigned p_; asm volatile("v_rcp_f32 %1, %1\n\tv_rcp_f32 %2, %2\n\tv_mul_f32 %1, %3, %1\n\tv_mul_f32 %2, %4, %2\n\tv_cvt_pk_bf16_f32 %0, %1, %2" \
    : "=v"(p_), "+v"(ez0), "+v"(ez1) : "v"(ev0), "v"(ev1)); ho[(a) >> 1][2 * ((a) & 1) + (hf_)] = p_; } while (0)
#define EP_PAIR(accv, a, hf_) do { \
    f32x2 mr2 = {mean_r, mean_r}, rs2_ = {rstd, rstd}; \
    f32x2 c_ = f32x2{e_c0[a][2 * (hf_)], e_c0[a][2 * (hf_) + 1]} + f32x2{e_c1[a][2 * (hf_)], e_c1[a][2 * (hf_) + 1]}; \
    f32x2 t_ = __builtin_elementwise_fma(mr2, c_, f32x2{e_b[a][2 * (hf_)], e_b[a][2 * (hf_) + 1]}); \
    f32x2 v_ = __builtin_elementwise_fma(f32x2{accv[4 * (a) + 2 * (hf_)], accv[4 * (a) + 2 * (hf_) + 1]}, rs2_, t_); \
    ho[(a) >> 1][2 * ((a) & 1) + (hf_)] = gelu2(v_, GK1, GK2); } while (0)
  unsigned hso = (unsigned)((m32 >> 4) * 48 * 1024 + (hh * 16 + (m32 & 15)) * 16);
  const char* hst = hg + (size_t)((4 * rb + 2 * mh) * 48 + 6 * cc + pr + 2) * 1024;
  asm volatile("" : "+s"(hst), "+v"(hso));
const char* flg = ws + WS_FLAGS + rb * 256 + cc * 32 + w * 8;
  asm volatile("" : "+s"(flg));
  u64 tag;
  asm volatile("global_load_dwordx2 %0, %1, %2 sc1" : "=v"(tag) : "v"(0), "s"(ws + WS_EPOCH) : "memory");
  auto publish = [&](int nchunks) {
    if (lane == 0) {
      u64 v = tag + EPOCH_STEP + (u64)nchunks;
      asm volatile("global_store_dwordx2 %0, %1, %2 sc1\n\ts_nop 1" :: "v"(0), "v"(v), "s"(flg) : "memory");
    }
  };
  const int rbA = (6 * rb2) >> 2, rbB = ((6 * rb2 + 5) >> 2) > 31 ? 31 : ((6 * rb2 + 5) >> 2);
  const unsigned foff = (unsigned)(WS_FLAGS + ((lane & 32) ? rbB : rbA) * 256 + (lane & 31) * 8);
  u64 fv = 0;
#define FLAG_LOAD() do { if (w == 0 && has2) asm volatile("global_load_dwordx2 %0, %1, %2 sc1" : "=v"(fv) : "v"(foff), "s"(ws) : "memory"); } while (0)
  auto flag_spin = [&](int nchunks) {
    for (unsigned spins = 0; spins < SPIN_CAP; ++spins) {
      const u64 d = fv - (tag + EPOCH_STEP);
      if (__all(d >= (u64)nchunks && d <= 3ull)) break;
      __builtin_amdgcn_s_sleep(1);
      asm volatile("global_load_dwordx2 %0, %1, %2 sc1\n\ts_waitcnt vmcnt(0)" : "=&v"(fv) : "v"(foff), "s"(ws) : "memory");
    }
  };
#define FLAG_CHECK(n, cnt) do { if (w == 0 && has2) { asm volatile("s_waitcnt vmcnt(" #cnt ")" : "+v"(fv) :: "memory"); flag_spin(n); } } while (0)
  u32x4 hf[36];
  const char* hb[3];
#pragma unroll
  for (int r = 0; r < 3; ++r) {
    int rt = 6 * rb2 + 3 * (w >> 1) + r; rt = rt > 127 ? 127 : rt;
    hb[r] = hg + (size_t)(rt * 48 + 24 * (w & 1)) * 1024 + 4096;
    asm volatile("" : "+s"(hb[r]));
  }
  unsigned hv[3] = {(unsigned)lane * 16u, (unsigned)lane * 16u + 8192u, (unsigned)lane * 16u + 16384u};
  asm volatile("" : "+v"(hv[0]), "+v"(hv[1]), "+v"(hv[2]));
#define LD_HF(slot, r, KS) asm volatile("global_load_dwordx4 %0, %1, %2 offset:%3 sc1" : "=v"(hf[slot]) : "v"(hv[(KS) >> 3]), "s"(hb[r]), "i"((((KS) & 7) - 4) * 1024) : "memory")
#define EPI_STORE(c, s) asm volatile("global_store_dwordx4 %0, %1, %2 offset:%3 sc1\n\ts_nop 1" :: "v"(hso), "v"(ho[s]), "s"(hst), "i"((c) * 2048 - 2048 + (s) * 512) : "memory")

  BAR();
  RD_X(xf[0], 0, 13); RD_W(0, 0, 0); RD_X(xf[1], 1, 13); RD_W(0, 1, 0); RD_X(xf[2], 2, 13); RD_W(0, 2, 0); RD_X(xf[3], 3, 13); RD_W(0, 3, 0);
  WAITLBAR();
  if ((xcd >> 1) == 0 || !PFSTAG) PF_W1();
  acc0 = mfma32(wf[0][0], xf[0], acc0); SCHED(); RD_X(xf[4], 0, 12); RD_W(1, 0, 1); SCHED();
  cs0 = mfma16(wf[0][0], SEL, cs0); SCHED(); RD_X(xf[5], 1, 12); RD_W(1, 1, 1); SCHED();
  rs = mfma16(SEL, xf[0], rs); SCHED(); RD_X(xf[6], 2, 12); RD_W(1, 2, 1); SCHED();
  sq16 = mfma32(xf[0], xf[0], sq16); SCHED(); RD_X(xf[7], 3, 12); RD_W(1, 3, 1); SCHED();
  acc0 = mfma32(wf[0][1], xf[1], acc0); SCHED(); SCHED();
  cs0 = mfma16(wf[0][1], SEL, cs0); SCHED(); SCHED();
  acc0 = mfma32(wf[0][2], xf[2], acc0); SCHED(); SCHED();
  rs = mfma16(SEL, xf[2], rs); SCHED(); SCHED();
  sq16 = mfma32(xf[2], xf[2], sq16); SCHED(); SCHED();
  acc0 = mfma32(wf[0][3], xf[3], acc0); SCHED(); SCHED();
  LGKM0();
  acc0 = mfma32(wf[1][0], xf[4], acc0); SCHED(); RD_X(xf[8], 0, 11); RD_W(0, 0, 2); SCHED();
  cs0 = mfma16(wf[1][0], SEL, cs0); SCHED(); RD_X(xf[9], 1, 11); RD_W(0, 1, 2); SCHED();
  rs = mfma16(SEL, xf[4], rs); SCHED(); RD_X(xf[10], 2, 11); RD_W(0, 2, 2); SCHED();
  sq16 = mfma32(xf[4], xf[4], sq16); SCHED(); RD_X(xf[11], 3, 11); RD_W(0, 3, 2); SCHED();
  acc0 = mfma32(wf[1][1], xf[5], acc0); SCHED(); SCHED();
  cs0 = mfma16(wf[1][1], SEL, cs0); SCHED(); SCHED();
  acc0 = mfma32(wf[1][2], xf[6], acc0); SCHED(); SCHED();
  rs = mfma16(SEL, xf[6], rs); SCHED(); SCHED();
  sq16 = mfma32(xf[6], xf[6], sq16); SCHED(); SCHED();
  acc0 = mfma32(wf[1][3], xf[7], acc0); SCHED(); SCHED();
  WAITLBAR();
  if ((xcd >> 1) == 1) PF_W1();
  acc0 = mfma32(wf[0][0], xf[8], acc0); SCHED(); RD_X(xf[12], 0, 7); RD_W(1, 0, 3); SCHED();
  cs0 = mfma16(wf[0][0], SEL, cs0); SCHED(); RD_X(xf[13], 1, 7); RD_W(1, 1, 3); SCHED();
  rs = mfma16(SEL, xf[8], rs); SCHED(); RD_X(xf[14], 2, 7); RD_W(1, 2, 3); SCHED();
  sq16 = mfma32(xf[8], xf[8], sq16); SCHED(); RD_X(xf[15], 3, 7); RD_W(1, 3, 3); SCHED();
  acc0 = mfma32(wf[0][1], xf[9], acc0); SCHED(); b1_cvt(); SCHED();
  cs0 = mfma16(wf[0][1], SEL, cs0); SCHED(); SCHED();
  acc0 = mfma32(wf[0][2], xf[10], acc0); SCHED(); SCHED();
  rs = mfma16(SEL, xf[10], rs); SCHED(); SCHED();
  sq16 = mfma32(xf[10], xf[10], sq16); SCHED(); SCHED();
  acc0 = mfma32(wf[0][3], xf[11], acc0); SCHED(); SCHED();
  LGKM0();
  acc0 = mfma32(wf[1][0], xf[12], acc0); SCHED(); RD_X(xf[16], 0, 6); RD_W(0, 0, 4); SCHED();
  cs0 = mfma16(wf[1][0], SEL, cs0); SCHED(); RD_X(xf[17], 1, 6); RD_W(0, 1, 4); SCHED();
  rs = mfma16(SEL, xf[12], rs); SCHED(); RD_X(xf[18], 2, 6); RD_W(0, 2, 4); SCHED();
  sq16 = mfma32(xf[12], xf[12], sq16); SCHED(); RD_X(xf[19], 3, 6); RD_W(0, 3, 4); SCHED();
  acc0 = mfma32(wf[1][1], xf[13], acc0); SCHED(); SCHED();
  cs0 = mfma16(wf[1][1], SEL, cs0); SCHED(); SCHED();
  acc0 = mfma32(wf[1][2], xf[14], acc0); SCHED(); SCHED();
  rs = mfma16(SEL, xf[14], rs); SCHED(); SCHED();
  sq16 = mfma32(xf[14], xf[14], sq16); SCHED(); SCHED();
  acc0 = mfma32(wf[1][3], xf[15], acc0); SCHED(); SCHED();
  WAITLBAR();
  if ((xcd >> 1) == 2) PF_W1();
  if ((xcd >> 1) == 0) PF_W2();
  acc0 = mfma32(wf[0][0], xf[16], acc0); SCHED(); RD_X(xh[1][0], 0, 18); RD_W(1, 0, 5); SCHED();
  cs0 = mfma16(wf[0][0], SEL, cs0); SCHED(); RD_X(xh[1][1], 1, 18); RD_W(1, 1, 5); SCHED();
  rs = mfma16(SEL, xf[16], rs); SCHED(); RD_X(xh[1][2], 2, 18); RD_W(1, 2, 5); SCHED();
  sq16 = mfma32(xf[16], xf[16], sq16); SCHED(); RD_X(xh[1][3], 3, 18); RD_W(1, 3, 5); SCHED();
  acc0 = mfma32(wf[0][1], xf[17], acc0); SCHED(); SCHED();
  cs0 = mfma16(wf[0][1], SEL, cs0); SCHED(); SCHED();
  acc0 = mfma32(wf[0][2], xf[18], acc0); SCHED(); SCHED();
  rs = mfma16(SEL, xf[18], rs); SCHED(); SCHED();
  sq16 = mfma32(xf[18], xf[18], sq16); SCHED(); SCHED();
  acc0 = mfma32(wf[0][3], xf[19], acc0); SCHED(); SCHED();
  LGKM0();
  acc0 = mfma32(wf[1][0], xh[1][0], acc0); SCHED(); RD_X(xh[0][0], 0, 17); RD_W(0, 0, 8); SCHED();
  cs0 = mfma16(wf[1][0], SEL, cs0); SCHED(); RD_X(xh[0][1], 1, 17); RD_W(0, 1, 8); SCHED();
  rs = mfma16(SEL, xh[1][0], rs); SCHED(); RD_X(xh[0][2], 2, 17); RD_W(0, 2, 8); SCHED();
  sq16 = mfma32(xh[1][0], xh[1][0], sq16); SCHED(); RD_X(xh[0][3], 3, 17); RD_W(0, 3, 8); SCHED();
  acc0 = mfma32(wf[1][1], xh[1][1], acc0); SCHED(); SCHED();
  cs0 = mfma16(wf[1][1], SEL, cs0); SCHED(); SCHED();
  acc0 = mfma32(wf[1][2], xh[1][2], acc0); SCHED(); SCHED();
  rs = mfma16(SEL, xh[1][2], rs); SCHED(); SCHED();
  sq16 = mfma32(xh[1][2], xh[1][2], sq16); SCHED(); SCHED();
  acc0 = mfma32(wf[1][3], xh[1][3], acc0); SCHED(); SCHED();
  WAITLBAR();
  if ((xcd >> 1) == 3) PF_W1();
  if ((xcd >> 1) == 1) PF_W2();
  acc0 = mfma32(wf[0][0], xh[0][0], acc0); SCHED(); RD_X(xh[1][0], 0, 16); RD_W(1, 0, 9); SCHED();
  cs0 = mfma16(wf[0][0], SEL, cs0); SCHED(); RD_X(xh[1][1], 1, 16); RD_W(1, 1, 9); SCHED();
  rs = mfma16(SEL, xh[0][0], rs); SCHED(); RD_X(xh[1][2], 2, 16); RD_W(1, 2, 9); SCHED();
  sq16 = mfma32(xh[0][0], xh[0][0], sq16); SCHED(); RD_X(xh[1][3], 3, 16); RD_W(1, 3, 9); SCHED();
  acc0 = mfma32(wf[0][1], xh[0][1], acc0); SCHED(); SCHED();
  cs0 = mfma16(wf[0][1], SEL, cs0); SCHED(); SCHED();
  acc0 = mfma32(wf[0][2], xh[0][2], acc0); SCHED(); SCHED();
  rs = mfma16(SEL, xh[0][2], rs); SCHED(); SCHED();
  sq16 = mfma32(xh[0][2], xh[0][2], sq16); SCHED(); SCHED();
  acc0 = mfma32(wf[0][3], xh[0][3], acc0); SCHED(); SCHED();
  LGKM0();
  acc0 = mfma32(wf[1][0], xh[1][0], acc0); SCHED(); RD_X(xh[0][0], 0, 15); RD_W(0, 0, 10); SCHED();
  cs0 = mfma16(wf[1][0], SEL, cs0); SCHED(); RD_X(xh[0][1], 1, 15); RD_W(0, 1, 10); SCHED();
  rs = mfma16(SEL, xh[1][0], rs); SCHED(); RD_X(xh[0][2], 2, 15); RD_W(0, 2, 10); SCHED();
  sq16 = mfma32(xh[1][0], xh[1][0], sq16); SCHED(); RD_X(xh[0][3], 3, 15); RD_W(0, 3, 10); SCHED();
  acc0 = mfma32(wf[1][1], xh[1][1], acc0); SCHED(); SCHED();
  cs0 = mfma16(wf[1][1], SEL, cs0); SCHED(); SCHED();
  acc0 = mfma32(wf[1][2], xh[1][2], acc0); SCHED(); SCHED();
  rs = mfma16(SEL, xh[1][2], rs); SCHED(); SCHED();
  sq16 = mfma32(xh[1][2], xh[1][2], sq16); SCHED(); SCHED();
  acc0 = mfma32(wf[1][3], xh[1][3], acc0); SCHED(); SCHED();
  WAITLBAR();
  if ((xcd >> 1) == 2) PF_W2();
  acc0 = mfma32(wf[0][0], xh[0][0], acc0); SCHED(); RD_X(xh[1][0], 0, 14); RD_W(1, 0, 0); SCHED();
  cs0 = mfma16(wf[0][0], SEL, cs0); SCHED(); RD_X(xh[1][1], 1, 14); RD_W(1, 1, 0); SCHED();
  rs = mfma16(SEL, xh[0][0], rs); SCHED(); RD_X(xh[1][2], 2, 14); RD_W(1, 2, 0); SCHED();
  sq16 = mfma32(xh[0][0], xh[0][0], sq16); SCHED(); RD_X(xh[1][3], 3, 14); RD_W(1, 3, 0); SCHED();
  acc0 = mfma32(wf[0][1], xh[0][1], acc0); SCHED(); SCHED();
  cs0 = mfma16(wf[0][1], SEL, cs0); SCHED(); SCHED();
  acc0 = mfma32(wf[0][2], xh[0][2], acc0); SCHED(); SCHED();
  rs = mfma16(SEL, xh[0][2], rs); SCHED(); SCHED();
  sq16 = mfma32(xh[0][2], xh[0][2], sq16); SCHED(); SCHED();
  acc0 = mfma32(wf[0][3], xh[0][3], acc0); SCHED(); SCHED();
  LGKM0();
  acc0 = mfma32(wf[1][0], xh[1][0], acc0); SCHED(); RD_W(0, 0, 13); SCHED();
  cs0 = mfma16(wf[1][0], SEL, cs0); SCHED(); RD_W(0, 1, 13); SCHED();
  rs = mfma16(SEL, xh[1][0], rs); SCHED(); RD_W(0, 2, 13); SCHED();
  sq16 = mfma32(xh[1][0], xh[1][0], sq16); SCHED(); RD_W(0, 3, 13); SCHED();
  acc0 = mfma32(wf[1][1], xh[1][1], acc0); SCHED(); SCHED();
  cs0 = mfma16(wf[1][1], SEL, cs0); SCHED(); SCHED();
  acc0 = mfma32(wf[1][2], xh[1][2], acc0); SCHED(); SCHED();
  rs = mfma16(SEL, xh[1][2], rs); SCHED(); SCHED();
  sq16 = mfma32(xh[1][2], xh[1][2], sq16); SCHED(); SCHED();
  acc0 = mfma32(wf[1][3], xh[1][3], acc0); SCHED(); SCHED();
  SEND_CS(0, cs0); send_st();
  WAITLBAR();
  if ((xcd >> 1) == 3) PF_W2();
  acc1 = mfma32(wf[0][0], xf[0], acc1); SCHED(); RD_W(1, 0, 1); EPI_LOAD(0, 0); EPI_LOAD(0, 1); epi_load_st(); SCHED();
  cs1 = mfma16(wf[0][0], SEL, cs1); SCHED(); RD_W(1, 1, 1); EPI_LOAD(0, 2); SCHED();
  acc1 = mfma32(wf[0][1], xf[1], acc1); SCHED(); RD_W(1, 2, 1); EPI_LOAD(0, 3); SCHED();
  cs1 = mfma16(wf[0][1], SEL, cs1); SCHED(); RD_W(1, 3, 1); SCHED();
  acc1 = mfma32(wf[0][2], xf[2], acc1); SCHED(); epi_stats(); SCHED();
  acc1 = mfma32(wf[0][3], xf[3], acc1); SCHED(); CS_ADD(0); E_S1(acc0, 0, 0); SCHED();
  LGKM0();
  acc1 = mfma32(wf[1][0], xf[4], acc1); SCHED(); RD_W(0, 0, 2); E_S2(); SCHED();
  cs1 = mfma16(wf[1][0], SEL, cs1); SCHED(); RD_W(0, 1, 2); SCHED();
  acc1 = mfma32(wf[1][1], xf[5], acc1); SCHED(); RD_W(0, 2, 2); E_S3X(); SCHED();
  cs1 = mfma16(wf[1][1], SEL, cs1); SCHED(); RD_W(0, 3, 2); SCHED();
  acc1 = mfma32(wf[1][2], xf[6], acc1); SCHED(); E_S4(0, 0); SCHED();
  acc1 = mfma32(wf[1][3], xf[7], acc1); SCHED(); E_S1(acc0, 0, 1); SCHED();
  WAITLBAR();
  acc1 = mfma32(wf[0][0], xf[8], acc1); SCHED(); RD_W(1, 0, 11); E_S2(); SCHED();
  cs1 = mfma16(wf[0][0], SEL, cs1); SCHED(); RD_W(1, 1, 11); SCHED();
  acc1 = mfma32(wf[0][1], xf[9], acc1); SCHED(); RD_W(1, 2, 11); E_S3X(); SCHED();
  cs1 = mfma16(wf[0][1], SEL, cs1); SCHED(); RD_W(1, 3, 11); SCHED();
  acc1 = mfma32(wf[0][2], xf[10], acc1); SCHED(); E_S4(0, 1); SCHED();
  acc1 = mfma32(wf[0][3], xf[11], acc1); SCHED(); CS_ADD(1); E_S1(acc0, 1, 0); SCHED();
  LGKM0();
  acc1 = mfma32(wf[1][0], xf[12], acc1); SCHED(); RD_W(0, 0, 12); E_S2(); SCHED();
  cs1 = mfma16(wf[1][0], SEL, cs1); SCHED(); RD_W(0, 1, 12); SCHED();
  acc1 = mfma32(wf[1][1], xf[13], acc1); SCHED(); RD_W(0, 2, 12); E_S3X(); SCHED();
  cs1 = mfma16(wf[1][1], SEL, cs1); SCHED(); RD_W(0, 3, 12); SCHED();
  acc1 = mfma32(wf[1][2], xf[14], acc1); SCHED(); E_S4(1, 0); SCHED();
  acc1 = mfma32(wf[1][3], xf[15], acc1); SCHED(); E_S1(acc0, 1, 1); SCHED();
  WAITLBAR();

  acc1 = mfma32(wf[0][0], xf[16], acc1); SCHED(); RD_X(xh[1][0], 0, 18); RD_W(1, 0, 3); E_S2(); SCHED();
  cs1 = mfma16(wf[0][0], SEL, cs1); SCHED(); RD_X(xh[1][1], 1, 18); RD_W(1, 1, 3); SCHED();
  acc1 = mfma32(wf[0][1], xf[17], acc1); SCHED(); RD_X(xh[1][2], 2, 18); RD_W(1, 2, 3); E_S3X(); SCHED();
  cs1 = mfma16(wf[0][1], SEL, cs1); SCHED(); RD_X(xh[1][3], 3, 18); RD_W(1, 3, 3); SCHED();
  acc1 = mfma32(wf[0][2], xf[18], acc1); SCHED(); E_S4(1, 1); EPI_STORE(0, 0); SCHED();
  acc1 = mfma32(wf[0][3], xf[19], acc1); SCHED(); CS_ADD(2); E_S1(acc0, 2, 0); SCHED();

  LGKM0();
  acc1 = mfma32(wf[1][0], xh[1][0], acc1); SCHED(); RD_X(xh[0][0], 0, 17); RD_W(0, 0, 4); E_S2(); SCHED();
  cs1 = mfma16(wf[1][0], SEL, cs1); SCHED(); RD_X(xh[0][1], 1, 17); RD_W(0, 1, 4); SCHED();
  acc1 = mfma32(wf[1][1], xh[1][1], acc1); SCHED(); RD_X(xh[0][2], 2, 17); RD_W(0, 2, 4); E_S3X(); SCHED();
  cs1 = mfma16(wf[1][1], SEL, cs1); SCHED(); RD_X(xh[0][3], 3, 17); RD_W(0, 3, 4); SCHED();
  acc1 = mfma32(wf[1][2], xh[1][2], acc1); SCHED(); E_S4(2, 0); SCHED();
  acc1 = mfma32(wf[1][3], xh[1][3], acc1); SCHED(); E_S1(acc0, 2, 1); SCHED();

  WAITLBAR();
  acc1 = mfma32(wf[0][0], xh[0][0], acc1); SCHED(); RD_X(xh[1][0], 0, 16); RD_W(1, 0, 6); E_S2(); SCHED();
  cs1 = mfma16(wf[0][0], SEL, cs1); SCHED(); RD_X(xh[1][1], 1, 16); RD_W(1, 1, 6); SCHED();
  acc1 = mfma32(wf[0][1], xh[0][1], acc1); SCHED(); RD_X(xh[1][2], 2, 16); RD_W(1, 2, 6); E_S3X(); SCHED();
  cs1 = mfma16(wf[0][1], SEL, cs1); SCHED(); RD_X(xh[1][3], 3, 16); RD_W(1, 3, 6); SCHED();
  acc1 = mfma32(wf[0][2], xh[0][2], acc1); SCHED(); E_S4(2, 1); SCHED();
  acc1 = mfma32(wf[0][3], xh[0][3], acc1); SCHED(); CS_ADD(3); E_S1(acc0, 3, 0); SCHED();
  LGKM0();
  acc1 = mfma32(wf[1][0], xh[1][0], acc1); SCHED(); RD_X(xh[0][0], 0, 15); RD_W(0, 0, 7); E_S2(); SCHED();
  cs1 = mfma16(wf[1][0], SEL, cs1); SCHED(); RD_X(xh[0][1], 1, 15); RD_W(0, 1, 7); SCHED();
  acc1 = mfma32(wf[1][1], xh[1][1], acc1); SCHED(); RD_X(xh[0][2], 2, 15); RD_W(0, 2, 7); E_S3X(); SCHED();
  cs1 = mfma16(wf[1][1], SEL, cs1); SCHED(); RD_X(xh[0][3], 3, 15); RD_W(0, 3, 7); SCHED();
  acc1 = mfma32(wf[1][2], xh[1][2], acc1); SCHED(); E_S4(3, 0); SCHED();
  acc1 = mfma32(wf[1][3], xh[1][3], acc1); SCHED(); E_S1(acc0, 3, 1); SCHED();
  WAITLBAR();
  acc1 = mfma32(wf[0][0], xh[0][0], acc1); SCHED(); RD_X(xh[1][0], 0, 14); RD_W(1, 0, 5); E_S2(); SCHED();
  cs1 = mfma16(wf[0][0], SEL, cs1); SCHED(); RD_X(xh[1][1], 1, 14); RD_W(1, 1, 5); SCHED();
  acc1 = mfma32(wf[0][1], xh[0][1], acc1); SCHED(); RD_X(xh[1][2], 2, 14); RD_W(1, 2, 5); E_S3X(); SCHED();
  cs1 = mfma16(wf[0][1], SEL, cs1); SCHED(); RD_X(xh[1][3], 3, 14); RD_W(1, 3, 5); SCHED();
  acc1 = mfma32(wf[0][2], xh[0][2], acc1); SCHED(); E_S4(3, 1); EPI_STORE(0, 1); SCHED();
  acc1 = mfma32(wf[0][3], xh[0][3], acc1); SCHED(); SCHED();
  LGKM0();
  acc1 = mfma32(wf[1][0], xh[1][0], acc1); SCHED(); RD_W(0, 0, 8); SCHED();
  cs1 = mfma16(wf[1][0], SEL, cs1); SCHED(); RD_W(0, 1, 8); SCHED();
  acc1 = mfma32(wf[1][1], xh[1][1], acc1); SCHED(); RD_W(0, 2, 8); SCHED();
  cs1 = mfma16(wf[1][1], SEL, cs1); SCHED(); RD_W(0, 3, 8); SCHED();
  acc1 = mfma32(wf[1][2], xh[1][2], acc1); SCHED(); SCHED();
  acc1 = mfma32(wf[1][3], xh[1][3], acc1); SCHED(); SCHED();
  SEND_CS(1, cs1);
  WAITLBAR();
  acc2_ = mfma32(wf[0][0], xf[0], acc2_); SCHED(); RD_W(1, 0, 9); EPI_LOAD(1, 0); EPI_LOAD(1, 1); SCHED();
  cs2 = mfma16(wf[0][0], SEL, cs2); SCHED(); RD_W(1, 1, 9); EPI_LOAD(1, 2); SCHED();
  acc2_ = mfma32(wf[0][1], xf[1], acc2_); SCHED(); RD_W(1, 2, 9); EPI_LOAD(1, 3); CS_ADD(0); SCHED();
  cs2 = mfma16(wf[0][1], SEL, cs2); SCHED(); RD_W(1, 3, 9); SCHED();
  acc2_ = mfma32(wf[0][2], xf[2], acc2_); SCHED(); E_S1(acc1, 0, 0); SCHED();
  acc2_ = mfma32(wf[0][3], xf[3], acc2_); SCHED(); E_S2(); SCHED();
  LGKM0();
  acc2_ = mfma32(wf[1][0], xf[4], acc2_); SCHED(); RD_W(0, 0, 10); E_S3X(); SCHED();
  cs2 = mfma16(wf[1][0], SEL, cs2); SCHED(); RD_W(0, 1, 10); SCHED();
  acc2_ = mfma32(wf[1][1], xf[5], acc2_); SCHED(); RD_W(0, 2, 10); E_S4(0, 0); SCHED();
  cs2 = mfma16(wf[1][1], SEL, cs2); SCHED(); RD_W(0, 3, 10); SCHED();
  acc2_ = mfma32(wf[1][2], xf[6], acc2_); SCHED(); E_S1(acc1, 0, 1); SCHED();
  acc2_ = mfma32(wf[1][3], xf[7], acc2_); SCHED(); E_S2(); SCHED();
  WAITLBAR();
  acc2_ = mfma32(wf[0][0], xf[8], acc2_); SCHED(); RD_W(1, 0, 0); E_S3X(); SCHED();
  cs2 = mfma16(wf[0][0], SEL, cs2); SCHED(); RD_W(1, 1, 0); SCHED();
  acc2_ = mfma32(wf[0][1], xf[9], acc2_); SCHED(); RD_W(1, 2, 0); E_S4(0, 1); SCHED();
  cs2 = mfma16(wf[0][1], SEL, cs2); SCHED(); RD_W(1, 3, 0); SCHED();
  acc2_ = mfma32(wf[0][2], xf[10], acc2_); SCHED(); CS_ADD(1); E_S1(acc1, 1, 0); SCHED();
  acc2_ = mfma32(wf[0][3], xf[11], acc2_); SCHED(); E_S2(); SCHED();
  LGKM0();
  acc2_ = mfma32(wf[1][0], xf[12], acc2_); SCHED(); RD_W(0, 0, 13); E_S3X(); SCHED();
  cs2 = mfma16(wf[1][0], SEL, cs2); SCHED(); RD_W(0, 1, 13); SCHED();
  acc2_ = mfma32(wf[1][1], xf[13], acc2_); SCHED(); RD_W(0, 2, 13); E_S4(1, 0); SCHED();
  cs2 = mfma16(wf[1][1], SEL, cs2); SCHED(); RD_W(0, 3, 13); SCHED();
  acc2_ = mfma32(wf[1][2], xf[14], acc2_); SCHED(); E_S1(acc1, 1, 1); SCHED();
  acc2_ = mfma32(wf[1][3], xf[15], acc2_); SCHED(); E_S2(); SCHED();
  WAIT_V(0); asm volatile("" :: "v"(pf_sink), "v"(pf_code)); asm volatile("" : "+v"(tag)); publish(1);
  WAITLBAR();
  acc2_ = mfma32(wf[0][0], xf[16], acc2_); SCHED(); RD_X(xh[1][0], 0, 18); RD_W(1, 0, 1); E_S3X(); SCHED();
  cs2 = mfma16(wf[0][0], SEL, cs2); SCHED(); RD_X(xh[1][1], 1, 18); RD_W(1, 1, 1); SCHED();
  acc2_ = mfma32(wf[0][1], xf[17], acc2_); SCHED(); RD_X(xh[1][2], 2, 18); RD_W(1, 2, 1); SCHED();
  cs2 = mfma16(wf[0][1], SEL, cs2); SCHED(); RD_X(xh[1][3], 3, 18); RD_W(1, 3, 1); SCHED();
  acc2_ = mfma32(wf[0][2], xf[18], acc2_); SCHED(); E_S4(1, 1); EPI_STORE(1, 0); SCHED();
  acc2_ = mfma32(wf[0][3], xf[19], acc2_); SCHED(); CS_ADD(2); E_S1(acc1, 2, 0); SCHED();
  LGKM0();
  acc2_ = mfma32(wf[1][0], xh[1][0], acc2_); SCHED(); RD_X(xh[0][0], 0, 17); RD_W(0, 0, 2); E_S2(); SCHED();
  cs2 = mfma16(wf[1][0], SEL, cs2); SCHED(); RD_X(xh[0][1], 1, 17); RD_W(0, 1, 2); SCHED();
  acc2_ = mfma32(wf[1][1], xh[1][1], acc2_); SCHED(); RD_X(xh[0][2], 2, 17); RD_W(0, 2, 2); E_S3X(); SCHED();
  cs2 = mfma16(wf[1][1], SEL, cs2); SCHED(); RD_X(xh[0][3], 3, 17); RD_W(0, 3, 2); SCHED();
  acc2_ = mfma32(wf[1][2], xh[1][2], acc2_); SCHED(); E_S4(2, 0); SCHED();
  acc2_ = mfma32(wf[1][3], xh[1][3], acc2_); SCHED(); E_S1(acc1, 2, 1); SCHED();
  WAITLBAR();
  acc2_ = mfma32(wf[0][0], xh[0][0], acc2_); SCHED(); RD_X(xh[1][0], 0, 16); RD_W(1, 0, 11); E_S2(); SCHED();
  cs2 = mfma16(wf[0][0], SEL, cs2); SCHED(); RD_X(xh[1][1], 1, 16); RD_W(1, 1, 11); SCHED();
  acc2_ = mfma32(wf[0][1], xh[0][1], acc2_); SCHED(); RD_X(xh[1][2], 2, 16); RD_W(1, 2, 11); E_S3X(); SCHED();
  cs2 = mfma16(wf[0][1], SEL, cs2); SCHED(); RD_X(xh[1][3], 3, 16); RD_W(1, 3, 11); SCHED();
  acc2_ = mfma32(wf[0][2], xh[0][2], acc2_); SCHED(); E_S4(2, 1); SCHED();
  acc2_ = mfma32(wf[0][3], xh[0][3], acc2_); SCHED(); CS_ADD(3); E_S1(acc1, 3, 0); SCHED();
  LGKM0();
  acc2_ = mfma32(wf[1][0], xh[1][0], acc2_); SCHED(); RD_X(xh[0][0], 0, 15); RD_W(0, 0, 12); E_S2(); SCHED();
  cs2 = mfma16(wf[1][0], SEL, cs2); SCHED(); RD_X(xh[0][1], 1, 15); RD_W(0, 1, 12); SCHED();
  acc2_ = mfma32(wf[1][1], xh[1][1], acc2_); SCHED(); RD_X(xh[0][2], 2, 15); RD_W(0, 2, 12); E_S3X(); SCHED();
  cs2 = mfma16(wf[1][1], SEL, cs2); SCHED(); RD_X(xh[0][3], 3, 15); RD_W(0, 3, 12); SCHED();
  acc2_ = mfma32(wf[1][2], xh[1][2], acc2_); SCHED(); E_S4(3, 0); SCHED();
  acc2_ = mfma32(wf[1][3], xh[1][3], acc2_); SCHED(); E_S1(acc1, 3, 1); SCHED();
  WAITLBAR();
  FLAG_LOAD();
  acc2_ = mfma32(wf[0][0], xh[0][0], acc2_); SCHED(); RD_X(xh[1][0], 0, 14); RD_W(1, 0, 3); E_S2(); SCHED();
  cs2 = mfma16(wf[0][0], SEL, cs2); SCHED(); RD_X(xh[1][1], 1, 14); RD_W(1, 1, 3); SCHED();
  acc2_ = mfma32(wf[0][1], xh[0][1], acc2_); SCHED(); RD_X(xh[1][2], 2, 14); RD_W(1, 2, 3); E_S3X(); SCHED();
  cs2 = mfma16(wf[0][1], SEL, cs2); SCHED(); RD_X(xh[1][3], 3, 14); RD_W(1, 3, 3); SCHED();
  acc2_ = mfma32(wf[0][2], xh[0][2], acc2_); SCHED(); E_S4(3, 1); EPI_STORE(1, 1); SCHED();
  acc2_ = mfma32(wf[0][3], xh[0][3], acc2_); SCHED(); SCHED();
  LGKM0();
  acc2_ = mfma32(wf[1][0], xh[1][0], acc2_); SCHED(); SCHED();
  cs2 = mfma16(wf[1][0], SEL, cs2); SCHED(); SCHED();
  acc2_ = mfma32(wf[1][1], xh[1][1], acc2_); SCHED(); SCHED();
  cs2 = mfma16(wf[1][1], SEL, cs2); SCHED(); SCHED();
  acc2_ = mfma32(wf[1][2], xh[1][2], acc2_); SCHED(); SCHED();
  acc2_ = mfma32(wf[1][3], xh[1][3], acc2_); SCHED(); SCHED();
  SEND_CS(2, cs2);
  FLAG_CHECK(1, 1); WAITLBAR();
  EPI_LOAD(2, 0); EPI_LOAD(2, 1); EPI_LOAD(2, 2); EPI_LOAD(2, 3);
  EP_PAIR(acc2_, 0, 0);
  EP_PAIR(acc2_, 0, 1);
  WAIT_V(0); publish(2);
  EP_PAIR(acc2_, 1, 0);
  EP_PAIR(acc2_, 1, 1);
  EPI_STORE(2, 0);
  EP_PAIR(acc2_, 2, 0);
  EP_PAIR(acc2_, 2, 1);
  EP_PAIR(acc2_, 3, 0);
  EP_PAIR(acc2_, 3, 1);
  EPI_STORE(2, 1);
  if (!has2) {
    WAIT_V(0); publish(3); BAR();
    return;
  }
  LD_HF(0, 0, 0); LD_HF(1, 1, 0); LD_HF(2, 2, 0);
  LD_HF(3, 0, 1); LD_HF(4, 1, 1); LD_HF(5, 2, 1);
  LD_HF(6, 0, 6); LD_HF(7, 1, 6); LD_HF(8, 2, 6);
  LD_HF(9, 0, 7); LD_HF(10, 1, 7); LD_HF(11, 2, 7);
  WAIT_V(12); publish(3);
  LD_HF(12, 0, 12); LD_HF(13, 1, 12); LD_HF(14, 2, 12);
  LD_HF(15, 0, 13); LD_HF(16, 1, 13); LD_HF(17, 2, 13);
  LD_HF(18, 0, 18); LD_HF(19, 1, 18); LD_HF(20, 2, 18);
  LD_HF(21, 0, 19); LD_HF(22, 1, 19); LD_HF(23, 2, 19);
  BAR();

  const int kh2 = w & 1, mh2 = w >> 1;
  int vaL[4], vaH[4];
  {
    const int r = 4 * (g & 1) + q;
#pragma unroll
    for (int t = 0; t < 4; ++t) {
      const int ct = t ^ (2 * kh2);
      vaL[t] = kh2 * 4096 + (g >> 1) * 2048 + (8 * r + ((2 * ct + (p >> 1)) ^ ((r >> 1) << 1))) * 16 + (p & 1) * 8;
      vaH[t] = vaL[t] + 65536;
      asm volatile("" : "+v"(vaL[t]), "+v"(vaH[t]));
    }
  }
  f32x4 a2[3][4];
#pragma unroll
  for (int r = 0; r < 3; ++r)
#pragma unroll
    for (int t = 0; t < 4; ++t) a2[r][t] = f32x4{0, 0, 0, 0};
  s16x8 vf[2][4];
#define RD_V(buf, t, unit) vf[buf][t] = trfrag(L + ((unit) < 8 ? vaL[t] : vaH[t]) + ((unit) & 7) * UNIT, 1024)
  u32x2 b2v;

  BAR();
  RD_V(0, 0, 4); RD_V(0, 1, 4); RD_V(0, 2, 4); RD_V(0, 3, 4);
  WAITLBAR();
  FLAG_LOAD();
  asm volatile("s_waitcnt vmcnt(22)" : "+v"(hf[0]), "+v"(hf[1]), "+v"(hf[2]) :: "memory"); SCHED();
  a2[0][0] = mfma16(vf[0][0], __builtin_bit_cast(s16x8, hf[0]), a2[0][0]); SCHED(); RD_V(1, 0, 6); SCHED();
  a2[1][0] = mfma16(vf[0][0], __builtin_bit_cast(s16x8, hf[1]), a2[1][0]); SCHED(); RD_V(1, 1, 6); SCHED();
  a2[2][0] = mfma16(vf[0][0], __builtin_bit_cast(s16x8, hf[2]), a2[2][0]); SCHED(); RD_V(1, 2, 6); SCHED();
  a2[0][1] = mfma16(vf[0][1], __builtin_bit_cast(s16x8, hf[0]), a2[0][1]); SCHED(); RD_V(1, 3, 6); SCHED();
  a2[1][1] = mfma16(vf[0][1], __builtin_bit_cast(s16x8, hf[1]), a2[1][1]); SCHED();
  a2[2][1] = mfma16(vf[0][1], __builtin_bit_cast(s16x8, hf[2]), a2[2][1]); SCHED();
  a2[0][2] = mfma16(vf[0][2], __builtin_bit_cast(s16x8, hf[0]), a2[0][2]); SCHED();
  a2[1][2] = mfma16(vf[0][2], __builtin_bit_cast(s16x8, hf[1]), a2[1][2]); SCHED();
  a2[2][2] = mfma16(vf[0][2], __builtin_bit_cast(s16x8, hf[2]), a2[2][2]); SCHED();
  a2[0][3] = mfma16(vf[0][3], __builtin_bit_cast(s16x8, hf[0]), a2[0][3]); SCHED();
  a2[1][3] = mfma16(vf[0][3], __builtin_bit_cast(s16x8, hf[1]), a2[1][3]); SCHED();
  a2[2][3] = mfma16(vf[0][3], __builtin_bit_cast(s16x8, hf[2]), a2[2][3]); SCHED();
  LGKM0();
  asm volatile("s_waitcnt vmcnt(19)" : "+v"(hf[3]), "+v"(hf[4]), "+v"(hf[5]) :: "memory"); SCHED();
  a2[0][0] = mfma16(vf[1][0], __builtin_bit_cast(s16x8, hf[3]), a2[0][0]); SCHED(); RD_V(0, 0, 7); SCHED();
  a2[1][0] = mfma16(vf[1][0], __builtin_bit_cast(s16x8, hf[4]), a2[1][0]); SCHED(); RD_V(0, 1, 7); SCHED();
  a2[2][0] = mfma16(vf[1][0], __builtin_bit_cast(s16x8, hf[5]), a2[2][0]); SCHED(); RD_V(0, 2, 7); SCHED();
  a2[0][1] = mfma16(vf[1][1], __builtin_bit_cast(s16x8, hf[3]), a2[0][1]); SCHED(); RD_V(0, 3, 7); SCHED();
  a2[1][1] = mfma16(vf[1][1], __builtin_bit_cast(s16x8, hf[4]), a2[1][1]); SCHED();
  a2[2][1] = mfma16(vf[1][1], __builtin_bit_cast(s16x8, hf[5]), a2[2][1]); SCHED();
  a2[0][2] = mfma16(vf[1][2], __builtin_bit_cast(s16x8, hf[3]), a2[0][2]); SCHED();
  a2[1][2] = mfma16(vf[1][2], __builtin_bit_cast(s16x8, hf[4]), a2[1][2]); SCHED();
  a2[2][2] = mfma16(vf[1][2], __builtin_bit_cast(s16x8, hf[5]), a2[2][2]); SCHED();
  a2[0][3] = mfma16(vf[1][3], __builtin_bit_cast(s16x8, hf[3]), a2[0][3]); SCHED();
  a2[1][3] = mfma16(vf[1][3], __builtin_bit_cast(s16x8, hf[4]), a2[1][3]); SCHED();
  a2[2][3] = mfma16(vf[1][3], __builtin_bit_cast(s16x8, hf[5]), a2[2][3]); SCHED();
  WAITLBAR();
  asm volatile("s_waitcnt vmcnt(16)" : "+v"(hf[6]), "+v"(hf[7]), "+v"(hf[8]) :: "memory"); SCHED();
  a2[0][0] = mfma16(vf[0][0], __builtin_bit_cast(s16x8, hf[6]), a2[0][0]); SCHED(); RD_V(1, 0, 5); SCHED();
  a2[1][0] = mfma16(vf[0][0], __builtin_bit_cast(s16x8, hf[7]), a2[1][0]); SCHED(); RD_V(1, 1, 5); SCHED();
  a2[2][0] = mfma16(vf[0][0], __builtin_bit_cast(s16x8, hf[8]), a2[2][0]); SCHED(); RD_V(1, 2, 5); SCHED();
  a2[0][1] = mfma16(vf[0][1], __builtin_bit_cast(s16x8, hf[6]), a2[0][1]); SCHED(); RD_V(1, 3, 5); SCHED();
  a2[1][1] = mfma16(vf[0][1], __builtin_bit_cast(s16x8, hf[7]), a2[1][1]); SCHED();
  a2[2][1] = mfma16(vf[0][1], __builtin_bit_cast(s16x8, hf[8]), a2[2][1]); SCHED();
  a2[0][2] = mfma16(vf[0][2], __builtin_bit_cast(s16x8, hf[6]), a2[0][2]); SCHED();
  a2[1][2] = mfma16(vf[0][2], __builtin_bit_cast(s16x8, hf[7]), a2[1][2]); SCHED();
  a2[2][2] = mfma16(vf[0][2], __builtin_bit_cast(s16x8, hf[8]), a2[2][2]); SCHED();
  a2[0][3] = mfma16(vf[0][3], __builtin_bit_cast(s16x8, hf[6]), a2[0][3]); SCHED();
  a2[1][3] = mfma16(vf[0][3], __builtin_bit_cast(s16x8, hf[7]), a2[1][3]); SCHED();
  a2[2][3] = mfma16(vf[0][3], __builtin_bit_cast(s16x8, hf[8]), a2[2][3]); SCHED();
  LGKM0();
  asm volatile("s_waitcnt vmcnt(13)" : "+v"(hf[9]), "+v"(hf[10]), "+v"(hf[11]) :: "memory"); SCHED();
  a2[0][0] = mfma16(vf[1][0], __builtin_bit_cast(s16x8, hf[9]), a2[0][0]); SCHED(); RD_V(0, 0, 8); SCHED();
  a2[1][0] = mfma16(vf[1][0], __builtin_bit_cast(s16x8, hf[10]), a2[1][0]); SCHED(); RD_V(0, 1, 8); SCHED();
  a2[2][0] = mfma16(vf[1][0], __builtin_bit_cast(s16x8, hf[11]), a2[2][0]); SCHED(); RD_V(0, 2, 8); SCHED();
  a2[0][1] = mfma16(vf[1][1], __builtin_bit_cast(s16x8, hf[9]), a2[0][1]); SCHED(); RD_V(0, 3, 8); SCHED();
  a2[1][1] = mfma16(vf[1][1], __builtin_bit_cast(s16x8, hf[10]), a2[1][1]); SCHED();
  a2[2][1] = mfma16(vf[1][1], __builtin_bit_cast(s16x8, hf[11]), a2[2][1]); SCHED();
  a2[0][2] = mfma16(vf[1][2], __builtin_bit_cast(s16x8, hf[9]), a2[0][2]); SCHED();
  a2[1][2] = mfma16(vf[1][2], __builtin_bit_cast(s16x8, hf[10]), a2[1][2]); SCHED();
  a2[2][2] = mfma16(vf[1][2], __builtin_bit_cast(s16x8, hf[11]), a2[2][2]); SCHED();
  a2[0][3] = mfma16(vf[1][3], __builtin_bit_cast(s16x8, hf[9]), a2[0][3]); SCHED();
  a2[1][3] = mfma16(vf[1][3], __builtin_bit_cast(s16x8, hf[10]), a2[1][3]); SCHED();
  a2[2][3] = mfma16(vf[1][3], __builtin_bit_cast(s16x8, hf[11]), a2[2][3]); SCHED();
  FLAG_CHECK(2, 0);
  WAITLBAR();
  asm volatile("s_waitcnt vmcnt(9)" : "+v"(hf[12]), "+v"(hf[13]), "+v"(hf[14]) :: "memory"); SCHED();
  a2[0][0] = mfma16(vf[0][0], __builtin_bit_cast(s16x8, hf[12]), a2[0][0]); SCHED(); RD_V(1, 0, 9); SCHED();
  a2[1][0] = mfma16(vf[0][0], __builtin_bit_cast(s16x8, hf[13]), a2[1][0]); SCHED(); RD_V(1, 1, 9); SCHED();
  a2[2][0] = mfma16(vf[0][0], __builtin_bit_cast(s16x8, hf[14]), a2[2][0]); SCHED(); RD_V(1, 2, 9); SCHED();
  a2[0][1] = mfma16(vf[0][1], __builtin_bit_cast(s16x8, hf[12]), a2[0][1]); SCHED(); RD_V(1, 3, 9); SCHED();
  a2[1][1] = mfma16(vf[0][1], __builtin_bit_cast(s16x8, hf[13]), a2[1][1]); SCHED(); LD_HF(24, 0, 2); SCHED();
  a2[2][1] = mfma16(vf[0][1], __builtin_bit_cast(s16x8, hf[14]), a2[2][1]); SCHED(); LD_HF(25, 1, 2); SCHED();
  a2[0][2] = mfma16(vf[0][2], __builtin_bit_cast(s16x8, hf[12]), a2[0][2]); SCHED(); LD_HF(26, 2, 2); SCHED();
  a2[1][2] = mfma16(vf[0][2], __builtin_bit_cast(s16x8, hf[13]), a2[1][2]); SCHED();
  a2[2][2] = mfma16(vf[0][2], __builtin_bit_cast(s16x8, hf[14]), a2[2][2]); SCHED();
  a2[0][3] = mfma16(vf[0][3], __builtin_bit_cast(s16x8, hf[12]), a2[0][3]); SCHED();
  a2[1][3] = mfma16(vf[0][3], __builtin_bit_cast(s16x8, hf[13]), a2[1][3]); SCHED();
  a2[2][3] = mfma16(vf[0][3], __builtin_bit_cast(s16x8, hf[14]), a2[2][3]); SCHED();
  LGKM0();
  asm volatile("s_waitcnt vmcnt(9)" : "+v"(hf[15]), "+v"(hf[16]), "+v"(hf[17]) :: "memory"); SCHED();
  a2[0][0] = mfma16(vf[1][0], __builtin_bit_cast(s16x8, hf[15]), a2[0][0]); SCHED(); RD_V(0, 0, 10); SCHED();
  a2[1][0] = mfma16(vf[1][0], __builtin_bit_cast(s16x8, hf[16]), a2[1][0]); SCHED(); RD_V(0, 1, 10); SCHED();
  a2[2][0] = mfma16(vf[1][0], __builtin_bit_cast(s16x8, hf[17]), a2[2][0]); SCHED(); RD_V(0, 2, 10); SCHED();
  a2[0][1] = mfma16(vf[1][1], __builtin_bit_cast(s16x8, hf[15]), a2[0][1]); SCHED(); RD_V(0, 3, 10); SCHED();
  a2[1][1] = mfma16(vf[1][1], __builtin_bit_cast(s16x8, hf[16]), a2[1][1]); SCHED(); LD_HF(27, 0, 3); SCHED();
  a2[2][1] = mfma16(vf[1][1], __builtin_bit_cast(s16x8, hf[17]), a2[2][1]); SCHED(); LD_HF(28, 1, 3); SCHED();
  a2[0][2] = mfma16(vf[1][2], __builtin_bit_cast(s16x8, hf[15]), a2[0][2]); SCHED(); LD_HF(29, 2, 3); SCHED();
  a2[1][2] = mfma16(vf[1][2], __builtin_bit_cast(s16x8, hf[16]), a2[1][2]); SCHED();
  a2[2][2] = mfma16(vf[1][2], __builtin_bit_cast(s16x8, hf[17]), a2[2][2]); SCHED();
  a2[0][3] = mfma16(vf[1][3], __builtin_bit_cast(s16x8, hf[15]), a2[0][3]); SCHED();
  a2[1][3] = mfma16(vf[1][3], __builtin_bit_cast(s16x8, hf[16]), a2[1][3]); SCHED();
  a2[2][3] = mfma16(vf[1][3], __builtin_bit_cast(s16x8, hf[17]), a2[2][3]); SCHED();
  WAITLBAR();
  asm volatile("s_waitcnt vmcnt(9)" : "+v"(hf[18]), "+v"(hf[19]), "+v"(hf[20]) :: "memory"); SCHED();
  a2[0][0] = mfma16(vf[0][0], __builtin_bit_cast(s16x8, hf[18]), a2[0][0]); SCHED(); RD_V(1, 0, 0); SCHED();
  a2[1][0] = mfma16(vf[0][0], __builtin_bit_cast(s16x8, hf[19]), a2[1][0]); SCHED(); RD_V(1, 1, 0); SCHED();
  a2[2][0] = mfma16(vf[0][0], __builtin_bit_cast(s16x8, hf[20]), a2[2][0]); SCHED(); RD_V(1, 2, 0); SCHED();
  a2[0][1] = mfma16(vf[0][1], __builtin_bit_cast(s16x8, hf[18]), a2[0][1]); SCHED(); RD_V(1, 3, 0); SCHED();
  a2[1][1] = mfma16(vf[0][1], __builtin_bit_cast(s16x8, hf[19]), a2[1][1]); SCHED(); LD_HF(30, 0, 8); SCHED();
  a2[2][1] = mfma16(vf[0][1], __builtin_bit_cast(s16x8, hf[20]), a2[2][1]); SCHED(); LD_HF(31, 1, 8); SCHED();
  a2[0][2] = mfma16(vf[0][2], __builtin_bit_cast(s16x8, hf[18]), a2[0][2]); SCHED(); LD_HF(32, 2, 8); SCHED();
  a2[1][2] = mfma16(vf[0][2], __builtin_bit_cast(s16x8, hf[19]), a2[1][2]); SCHED();
  a2[2][2] = mfma16(vf[0][2], __builtin_bit_cast(s16x8, hf[20]), a2[2][2]); SCHED();
  a2[0][3] = mfma16(vf[0][3], __builtin_bit_cast(s16x8, hf[18]), a2[0][3]); SCHED();
  a2[1][3] = mfma16(vf[0][3], __builtin_bit_cast(s16x8, hf[19]), a2[1][3]); SCHED();
  a2[2][3] = mfma16(vf[0][3], __builtin_bit_cast(s16x8, hf[20]), a2[2][3]); SCHED();
  LGKM0();
  asm volatile("s_waitcnt vmcnt(9)" : "+v"(hf[21]), "+v"(hf[22]), "+v"(hf[23]) :: "memory"); SCHED();
  a2[0][0] = mfma16(vf[1][0], __builtin_bit_cast(s16x8, hf[21]), a2[0][0]); SCHED(); RD_V(0, 0, 13); SCHED();
  a2[1][0] = mfma16(vf[1][0], __builtin_bit_cast(s16x8, hf[22]), a2[1][0]); SCHED(); RD_V(0, 1, 13); SCHED();
  a2[2][0] = mfma16(vf[1][0], __builtin_bit_cast(s16x8, hf[23]), a2[2][0]); SCHED(); RD_V(0, 2, 13); SCHED();
  a2[0][1] = mfma16(vf[1][1], __builtin_bit_cast(s16x8, hf[21]), a2[0][1]); SCHED(); RD_V(0, 3, 13); SCHED();
  a2[1][1] = mfma16(vf[1][1], __builtin_bit_cast(s16x8, hf[22]), a2[1][1]); SCHED(); LD_HF(33, 0, 9); SCHED();
  a2[2][1] = mfma16(vf[1][1], __builtin_bit_cast(s16x8, hf[23]), a2[2][1]); SCHED(); LD_HF(34, 1, 9); SCHED();
  a2[0][2] = mfma16(vf[1][2], __builtin_bit_cast(s16x8, hf[21]), a2[0][2]); SCHED(); LD_HF(35, 2, 9); SCHED();
  a2[1][2] = mfma16(vf[1][2], __builtin_bit_cast(s16x8, hf[22]), a2[1][2]); SCHED();
  a2[2][2] = mfma16(vf[1][2], __builtin_bit_cast(s16x8, hf[23]), a2[2][2]); SCHED();
  a2[0][3] = mfma16(vf[1][3], __builtin_bit_cast(s16x8, hf[21]), a2[0][3]); SCHED();
  a2[1][3] = mfma16(vf[1][3], __builtin_bit_cast(s16x8, hf[22]), a2[1][3]); SCHED();
  a2[2][3] = mfma16(vf[1][3], __builtin_bit_cast(s16x8, hf[23]), a2[2][3]); SCHED();
  WAITLBAR();
  FLAG_LOAD();
  asm volatile("s_waitcnt vmcnt(9)" : "+v"(hf[24]), "+v"(hf[25]), "+v"(hf[26]) :: "memory"); SCHED();
  a2[0][0] = mfma16(vf[0][0], __builtin_bit_cast(s16x8, hf[24]), a2[0][0]); SCHED(); RD_V(1, 0, 1); SCHED();
  a2[1][0] = mfma16(vf[0][0], __builtin_bit_cast(s16x8, hf[25]), a2[1][0]); SCHED(); RD_V(1, 1, 1); SCHED();
  a2[2][0] = mfma16(vf[0][0], __builtin_bit_cast(s16x8, hf[26]), a2[2][0]); SCHED(); RD_V(1, 2, 1); SCHED();
  a2[0][1] = mfma16(vf[0][1], __builtin_bit_cast(s16x8, hf[24]), a2[0][1]); SCHED(); RD_V(1, 3, 1); SCHED();
  a2[1][1] = mfma16(vf[0][1], __builtin_bit_cast(s16x8, hf[25]), a2[1][1]); SCHED(); LD_HF(0, 0, 14); SCHED();
  a2[2][1] = mfma16(vf[0][1], __builtin_bit_cast(s16x8, hf[26]), a2[2][1]); SCHED(); LD_HF(1, 1, 14); SCHED();
  a2[0][2] = mfma16(vf[0][2], __builtin_bit_cast(s16x8, hf[24]), a2[0][2]); SCHED(); LD_HF(2, 2, 14); SCHED();
  a2[1][2] = mfma16(vf[0][2], __builtin_bit_cast(s16x8, hf[25]), a2[1][2]); SCHED();
  a2[2][2] = mfma16(vf[0][2], __builtin_bit_cast(s16x8, hf[26]), a2[2][2]); SCHED();
  a2[0][3] = mfma16(vf[0][3], __builtin_bit_cast(s16x8, hf[24]), a2[0][3]); SCHED();
  a2[1][3] = mfma16(vf[0][3], __builtin_bit_cast(s16x8, hf[25]), a2[1][3]); SCHED();
  a2[2][3] = mfma16(vf[0][3], __builtin_bit_cast(s16x8, hf[26]), a2[2][3]); SCHED();
  LGKM0();
  asm volatile("s_waitcnt vmcnt(9)" : "+v"(hf[27]), "+v"(hf[28]), "+v"(hf[29]) :: "memory"); SCHED();
  a2[0][0] = mfma16(vf[1][0], __builtin_bit_cast(s16x8, hf[27]), a2[0][0]); SCHED(); RD_V(0, 0, 2); SCHED();
  a2[1][0] = mfma16(vf[1][0], __builtin_bit_cast(s16x8, hf[28]), a2[1][0]); SCHED(); RD_V(0, 1, 2); SCHED();
  a2[2][0] = mfma16(vf[1][0], __builtin_bit_cast(s16x8, hf[29]), a2[2][0]); SCHED(); RD_V(0, 2, 2); SCHED();
  a2[0][1] = mfma16(vf[1][1], __builtin_bit_cast(s16x8, hf[27]), a2[0][1]); SCHED(); RD_V(0, 3, 2); SCHED();
  a2[1][1] = mfma16(vf[1][1], __builtin_bit_cast(s16x8, hf[28]), a2[1][1]); SCHED(); LD_HF(3, 0, 15); SCHED();
  a2[2][1] = mfma16(vf[1][1], __builtin_bit_cast(s16x8, hf[29]), a2[2][1]); SCHED(); LD_HF(4, 1, 15); SCHED();
  a2[0][2] = mfma16(vf[1][2], __builtin_bit_cast(s16x8, hf[27]), a2[0][2]); SCHED(); LD_HF(5, 2, 15); SCHED();
  a2[1][2] = mfma16(vf[1][2], __builtin_bit_cast(s16x8, hf[28]), a2[1][2]); SCHED();
  a2[2][2] = mfma16(vf[1][2], __builtin_bit_cast(s16x8, hf[29]), a2[2][2]); SCHED();
  a2[0][3] = mfma16(vf[1][3], __builtin_bit_cast(s16x8, hf[27]), a2[0][3]); SCHED();
  a2[1][3] = mfma16(vf[1][3], __builtin_bit_cast(s16x8, hf[28]), a2[1][3]); SCHED();
  a2[2][3] = mfma16(vf[1][3], __builtin_bit_cast(s16x8, hf[29]), a2[2][3]); SCHED();
  FLAG_CHECK(3, 6);
  WAITLBAR();
  asm volatile("s_waitcnt vmcnt(9)" : "+v"(hf[30]), "+v"(hf[31]), "+v"(hf[32]) :: "memory"); SCHED();
  a2[0][0] = mfma16(vf[0][0], __builtin_bit_cast(s16x8, hf[30]), a2[0][0]); SCHED(); RD_V(1, 0, 11); SCHED();
  a2[1][0] = mfma16(vf[0][0], __builtin_bit_cast(s16x8, hf[31]), a2[1][0]); SCHED(); RD_V(1, 1, 11); SCHED();
  a2[2][0] = mfma16(vf[0][0], __builtin_bit_cast(s16x8, hf[32]), a2[2][0]); SCHED(); RD_V(1, 2, 11); SCHED();
  a2[0][1] = mfma16(vf[0][1], __builtin_bit_cast(s16x8, hf[30]), a2[0][1]); SCHED(); RD_V(1, 3, 11); SCHED();
  a2[1][1] = mfma16(vf[0][1], __builtin_bit_cast(s16x8, hf[31]), a2[1][1]); SCHED(); LD_HF(6, 0, 20); SCHED();
  a2[2][1] = mfma16(vf[0][1], __builtin_bit_cast(s16x8, hf[32]), a2[2][1]); SCHED(); LD_HF(7, 1, 20); SCHED();
  a2[0][2] = mfma16(vf[0][2], __builtin_bit_cast(s16x8, hf[30]), a2[0][2]); SCHED(); LD_HF(8, 2, 20); SCHED();
  a2[1][2] = mfma16(vf[0][2], __builtin_bit_cast(s16x8, hf[31]), a2[1][2]); SCHED();
  a2[2][2] = mfma16(vf[0][2], __builtin_bit_cast(s16x8, hf[32]), a2[2][2]); SCHED();
  a2[0][3] = mfma16(vf[0][3], __builtin_bit_cast(s16x8, hf[30]), a2[0][3]); SCHED();
  a2[1][3] = mfma16(vf[0][3], __builtin_bit_cast(s16x8, hf[31]), a2[1][3]); SCHED();
  a2[2][3] = mfma16(vf[0][3], __builtin_bit_cast(s16x8, hf[32]), a2[2][3]); SCHED();
  LGKM0();
  asm volatile("s_waitcnt vmcnt(9)" : "+v"(hf[33]), "+v"(hf[34]), "+v"(hf[35]) :: "memory"); SCHED();
  a2[0][0] = mfma16(vf[1][0], __builtin_bit_cast(s16x8, hf[33]), a2[0][0]); SCHED(); RD_V(0, 0, 12); SCHED();
  a2[1][0] = mfma16(vf[1][0], __builtin_bit_cast(s16x8, hf[34]), a2[1][0]); SCHED(); RD_V(0, 1, 12); SCHED();
  a2[2][0] = mfma16(vf[1][0], __builtin_bit_cast(s16x8, hf[35]), a2[2][0]); SCHED(); RD_V(0, 2, 12); SCHED();
  a2[0][1] = mfma16(vf[1][1], __builtin_bit_cast(s16x8, hf[33]), a2[0][1]); SCHED(); RD_V(0, 3, 12); SCHED();
  a2[1][1] = mfma16(vf[1][1], __builtin_bit_cast(s16x8, hf[34]), a2[1][1]); SCHED(); LD_HF(9, 0, 21); SCHED();
  a2[2][1] = mfma16(vf[1][1], __builtin_bit_cast(s16x8, hf[35]), a2[2][1]); SCHED(); LD_HF(10, 1, 21); SCHED();
  a2[0][2] = mfma16(vf[1][2], __builtin_bit_cast(s16x8, hf[33]), a2[0][2]); SCHED(); LD_HF(11, 2, 21); SCHED();
  a2[1][2] = mfma16(vf[1][2], __builtin_bit_cast(s16x8, hf[34]), a2[1][2]); SCHED();
  a2[2][2] = mfma16(vf[1][2], __builtin_bit_cast(s16x8, hf[35]), a2[2][2]); SCHED();
  a2[0][3] = mfma16(vf[1][3], __builtin_bit_cast(s16x8, hf[33]), a2[0][3]); SCHED();
  a2[1][3] = mfma16(vf[1][3], __builtin_bit_cast(s16x8, hf[34]), a2[1][3]); SCHED();
  a2[2][3] = mfma16(vf[1][3], __builtin_bit_cast(s16x8, hf[35]), a2[2][3]); SCHED();
  WAITLBAR();
  asm volatile("s_waitcnt vmcnt(9)" : "+v"(hf[0]), "+v"(hf[1]), "+v"(hf[2]) :: "memory"); SCHED();
  a2[0][0] = mfma16(vf[0][0], __builtin_bit_cast(s16x8, hf[0]), a2[0][0]); SCHED(); RD_V(1, 0, 15); SCHED();
  a2[1][0] = mfma16(vf[0][0], __builtin_bit_cast(s16x8, hf[1]), a2[1][0]); SCHED(); RD_V(1, 1, 15); SCHED();
  a2[2][0] = mfma16(vf[0][0], __builtin_bit_cast(s16x8, hf[2]), a2[2][0]); SCHED(); RD_V(1, 2, 15); SCHED();
  a2[0][1] = mfma16(vf[0][1], __builtin_bit_cast(s16x8, hf[0]), a2[0][1]); SCHED(); RD_V(1, 3, 15); SCHED();
  a2[1][1] = mfma16(vf[0][1], __builtin_bit_cast(s16x8, hf[1]), a2[1][1]); SCHED(); LD_HF(12, 0, 4); SCHED();
  a2[2][1] = mfma16(vf[0][1], __builtin_bit_cast(s16x8, hf[2]), a2[2][1]); SCHED(); LD_HF(13, 1, 4); SCHED();
  a2[0][2] = mfma16(vf[0][2], __builtin_bit_cast(s16x8, hf[0]), a2[0][2]); SCHED(); LD_HF(14, 2, 4); SCHED();
  a2[1][2] = mfma16(vf[0][2], __builtin_bit_cast(s16x8, hf[1]), a2[1][2]); SCHED();
  a2[2][2] = mfma16(vf[0][2], __builtin_bit_cast(s16x8, hf[2]), a2[2][2]); SCHED();
  a2[0][3] = mfma16(vf[0][3], __builtin_bit_cast(s16x8, hf[0]), a2[0][3]); SCHED();
  a2[1][3] = mfma16(vf[0][3], __builtin_bit_cast(s16x8, hf[1]), a2[1][3]); SCHED();
  a2[2][3] = mfma16(vf[0][3], __builtin_bit_cast(s16x8, hf[2]), a2[2][3]); SCHED();
  LGKM0();
  asm volatile("s_waitcnt vmcnt(9)" : "+v"(hf[3]), "+v"(hf[4]), "+v"(hf[5]) :: "memory"); SCHED();
  a2[0][0] = mfma16(vf[1][0], __builtin_bit_cast(s16x8, hf[3]), a2[0][0]); SCHED(); RD_V(0, 0, 3); SCHED();
  a2[1][0] = mfma16(vf[1][0], __builtin_bit_cast(s16x8, hf[4]), a2[1][0]); SCHED(); RD_V(0, 1, 3); SCHED();
  a2[2][0] = mfma16(vf[1][0], __builtin_bit_cast(s16x8, hf[5]), a2[2][0]); SCHED(); RD_V(0, 2, 3); SCHED();
  a2[0][1] = mfma16(vf[1][1], __builtin_bit_cast(s16x8, hf[3]), a2[0][1]); SCHED(); RD_V(0, 3, 3); SCHED();
  a2[1][1] = mfma16(vf[1][1], __builtin_bit_cast(s16x8, hf[4]), a2[1][1]); SCHED(); LD_HF(15, 0, 5); SCHED();
  a2[2][1] = mfma16(vf[1][1], __builtin_bit_cast(s16x8, hf[5]), a2[2][1]); SCHED(); LD_HF(16, 1, 5); SCHED();
  a2[0][2] = mfma16(vf[1][2], __builtin_bit_cast(s16x8, hf[3]), a2[0][2]); SCHED(); LD_HF(17, 2, 5); SCHED();
  a2[1][2] = mfma16(vf[1][2], __builtin_bit_cast(s16x8, hf[4]), a2[1][2]); SCHED();
  a2[2][2] = mfma16(vf[1][2], __builtin_bit_cast(s16x8, hf[5]), a2[2][2]); SCHED();
  a2[0][3] = mfma16(vf[1][3], __builtin_bit_cast(s16x8, hf[3]), a2[0][3]); SCHED();
  a2[1][3] = mfma16(vf[1][3], __builtin_bit_cast(s16x8, hf[4]), a2[1][3]); SCHED();
  a2[2][3] = mfma16(vf[1][3], __builtin_bit_cast(s16x8, hf[5]), a2[2][3]); SCHED();
  WAITLBAR();
  asm volatile("s_waitcnt vmcnt(9)" : "+v"(hf[6]), "+v"(hf[7]), "+v"(hf[8]) :: "memory"); SCHED();
  a2[0][0] = mfma16(vf[0][0], __builtin_bit_cast(s16x8, hf[6]), a2[0][0]); SCHED(); RD_V(1, 0, 14); SCHED();
  a2[1][0] = mfma16(vf[0][0], __builtin_bit_cast(s16x8, hf[7]), a2[1][0]); SCHED(); RD_V(1, 1, 14); SCHED();
  a2[2][0] = mfma16(vf[0][0], __builtin_bit_cast(s16x8, hf[8]), a2[2][0]); SCHED(); RD_V(1, 2, 14); SCHED();
  a2[0][1] = mfma16(vf[0][1], __builtin_bit_cast(s16x8, hf[6]), a2[0][1]); SCHED(); RD_V(1, 3, 14); SCHED();
  a2[1][1] = mfma16(vf[0][1], __builtin_bit_cast(s16x8, hf[7]), a2[1][1]); SCHED(); LD_HF(18, 0, 10); SCHED();
  a2[2][1] = mfma16(vf[0][1], __builtin_bit_cast(s16x8, hf[8]), a2[2][1]); SCHED(); LD_HF(19, 1, 10); SCHED();
  a2[0][2] = mfma16(vf[0][2], __builtin_bit_cast(s16x8, hf[6]), a2[0][2]); SCHED(); LD_HF(20, 2, 10); SCHED();
  a2[1][2] = mfma16(vf[0][2], __builtin_bit_cast(s16x8, hf[7]), a2[1][2]); SCHED();
  a2[2][2] = mfma16(vf[0][2], __builtin_bit_cast(s16x8, hf[8]), a2[2][2]); SCHED();
  a2[0][3] = mfma16(vf[0][3], __builtin_bit_cast(s16x8, hf[6]), a2[0][3]); SCHED();
  a2[1][3] = mfma16(vf[0][3], __builtin_bit_cast(s16x8, hf[7]), a2[1][3]); SCHED();
  a2[2][3] = mfma16(vf[0][3], __builtin_bit_cast(s16x8, hf[8]), a2[2][3]); SCHED();
  LGKM0();
  asm volatile("s_waitcnt vmcnt(9)" : "+v"(hf[9]), "+v"(hf[10]), "+v"(hf[11]) :: "memory"); SCHED();
  a2[0][0] = mfma16(vf[1][0], __builtin_bit_cast(s16x8, hf[9]), a2[0][0]); SCHED(); RD_V(0, 0, 4); SCHED();
  a2[1][0] = mfma16(vf[1][0], __builtin_bit_cast(s16x8, hf[10]), a2[1][0]); SCHED(); RD_V(0, 1, 4); SCHED();
  a2[2][0] = mfma16(vf[1][0], __builtin_bit_cast(s16x8, hf[11]), a2[2][0]); SCHED(); RD_V(0, 2, 4); SCHED();
  a2[0][1] = mfma16(vf[1][1], __builtin_bit_cast(s16x8, hf[9]), a2[0][1]); SCHED(); RD_V(0, 3, 4); SCHED();
  a2[1][1] = mfma16(vf[1][1], __builtin_bit_cast(s16x8, hf[10]), a2[1][1]); SCHED(); LD_HF(21, 0, 11); SCHED();
  a2[2][1] = mfma16(vf[1][1], __builtin_bit_cast(s16x8, hf[11]), a2[2][1]); SCHED(); LD_HF(22, 1, 11); SCHED();
  a2[0][2] = mfma16(vf[1][2], __builtin_bit_cast(s16x8, hf[9]), a2[0][2]); SCHED(); LD_HF(23, 2, 11); SCHED();
  a2[1][2] = mfma16(vf[1][2], __builtin_bit_cast(s16x8, hf[10]), a2[1][2]); SCHED();
  a2[2][2] = mfma16(vf[1][2], __builtin_bit_cast(s16x8, hf[11]), a2[2][2]); SCHED();
  a2[0][3] = mfma16(vf[1][3], __builtin_bit_cast(s16x8, hf[9]), a2[0][3]); SCHED();
  a2[1][3] = mfma16(vf[1][3], __builtin_bit_cast(s16x8, hf[10]), a2[1][3]); SCHED();
  a2[2][3] = mfma16(vf[1][3], __builtin_bit_cast(s16x8, hf[11]), a2[2][3]); SCHED();
  WAITLBAR();
  asm volatile("s_waitcnt vmcnt(9)" : "+v"(hf[12]), "+v"(hf[13]), "+v"(hf[14]) :: "memory"); SCHED();
  a2[0][0] = mfma16(vf[0][0], __builtin_bit_cast(s16x8, hf[12]), a2[0][0]); SCHED(); RD_V(1, 0, 6); SCHED();
  a2[1][0] = mfma16(vf[0][0], __builtin_bit_cast(s16x8, hf[13]), a2[1][0]); SCHED(); RD_V(1, 1, 6); SCHED();
  a2[2][0] = mfma16(vf[0][0], __builtin_bit_cast(s16x8, hf[14]), a2[2][0]); SCHED(); RD_V(1, 2, 6); SCHED();
  a2[0][1] = mfma16(vf[0][1], __builtin_bit_cast(s16x8, hf[12]), a2[0][1]); SCHED(); RD_V(1, 3, 6); SCHED();
  a2[1][1] = mfma16(vf[0][1], __builtin_bit_cast(s16x8, hf[13]), a2[1][1]); SCHED(); LD_HF(24, 0, 16); SCHED();
  a2[2][1] = mfma16(vf[0][1], __builtin_bit_cast(s16x8, hf[14]), a2[2][1]); SCHED(); LD_HF(25, 1, 16); SCHED();
  a2[0][2] = mfma16(vf[0][2], __builtin_bit_cast(s16x8, hf[12]), a2[0][2]); SCHED(); LD_HF(26, 2, 16); SCHED();
  a2[1][2] = mfma16(vf[0][2], __builtin_bit_cast(s16x8, hf[13]), a2[1][2]); SCHED();
  a2[2][2] = mfma16(vf[0][2], __builtin_bit_cast(s16x8, hf[14]), a2[2][2]); SCHED();
  a2[0][3] = mfma16(vf[0][3], __builtin_bit_cast(s16x8, hf[12]), a2[0][3]); SCHED();
  a2[1][3] = mfma16(vf[0][3], __builtin_bit_cast(s16x8, hf[13]), a2[1][3]); SCHED();
  a2[2][3] = mfma16(vf[0][3], __builtin_bit_cast(s16x8, hf[14]), a2[2][3]); SCHED();
  LGKM0();
  asm volatile("s_waitcnt vmcnt(9)" : "+v"(hf[15]), "+v"(hf[16]), "+v"(hf[17]) :: "memory"); SCHED();
  a2[0][0] = mfma16(vf[1][0], __builtin_bit_cast(s16x8, hf[15]), a2[0][0]); SCHED(); RD_V(0, 0, 7); SCHED();
  a2[1][0] = mfma16(vf[1][0], __builtin_bit_cast(s16x8, hf[16]), a2[1][0]); SCHED(); RD_V(0, 1, 7); SCHED();
  a2[2][0] = mfma16(vf[1][0], __builtin_bit_cast(s16x8, hf[17]), a2[2][0]); SCHED(); RD_V(0, 2, 7); SCHED();
  a2[0][1] = mfma16(vf[1][1], __builtin_bit_cast(s16x8, hf[15]), a2[0][1]); SCHED(); RD_V(0, 3, 7); SCHED();
  a2[1][1] = mfma16(vf[1][1], __builtin_bit_cast(s16x8, hf[16]), a2[1][1]); SCHED(); LD_HF(27, 0, 17); SCHED();
  a2[2][1] = mfma16(vf[1][1], __builtin_bit_cast(s16x8, hf[17]), a2[2][1]); SCHED(); LD_HF(28, 1, 17); SCHED();
  a2[0][2] = mfma16(vf[1][2], __builtin_bit_cast(s16x8, hf[15]), a2[0][2]); SCHED(); LD_HF(29, 2, 17); SCHED();
  a2[1][2] = mfma16(vf[1][2], __builtin_bit_cast(s16x8, hf[16]), a2[1][2]); SCHED();
  a2[2][2] = mfma16(vf[1][2], __builtin_bit_cast(s16x8, hf[17]), a2[2][2]); SCHED();
  a2[0][3] = mfma16(vf[1][3], __builtin_bit_cast(s16x8, hf[15]), a2[0][3]); SCHED();
  a2[1][3] = mfma16(vf[1][3], __builtin_bit_cast(s16x8, hf[16]), a2[1][3]); SCHED();
  a2[2][3] = mfma16(vf[1][3], __builtin_bit_cast(s16x8, hf[17]), a2[2][3]); SCHED();
  WAITLBAR();
  asm volatile("s_waitcnt vmcnt(9)" : "+v"(hf[18]), "+v"(hf[19]), "+v"(hf[20]) :: "memory"); SCHED();
  a2[0][0] = mfma16(vf[0][0], __builtin_bit_cast(s16x8, hf[18]), a2[0][0]); SCHED(); RD_V(1, 0, 5); SCHED();
  a2[1][0] = mfma16(vf[0][0], __builtin_bit_cast(s16x8, hf[19]), a2[1][0]); SCHED(); RD_V(1, 1, 5); SCHED();
  a2[2][0] = mfma16(vf[0][0], __builtin_bit_cast(s16x8, hf[20]), a2[2][0]); SCHED(); RD_V(1, 2, 5); SCHED();
  a2[0][1] = mfma16(vf[0][1], __builtin_bit_cast(s16x8, hf[18]), a2[0][1]); SCHED(); RD_V(1, 3, 5); SCHED();
  a2[1][1] = mfma16(vf[0][1], __builtin_bit_cast(s16x8, hf[19]), a2[1][1]); SCHED(); LD_HF(30, 0, 22); SCHED();
  a2[2][1] = mfma16(vf[0][1], __builtin_bit_cast(s16x8, hf[20]), a2[2][1]); SCHED(); LD_HF(31, 1, 22); SCHED();
  a2[0][2] = mfma16(vf[0][2], __builtin_bit_cast(s16x8, hf[18]), a2[0][2]); SCHED(); LD_HF(32, 2, 22); SCHED();
  a2[1][2] = mfma16(vf[0][2], __builtin_bit_cast(s16x8, hf[19]), a2[1][2]); SCHED();
  a2[2][2] = mfma16(vf[0][2], __builtin_bit_cast(s16x8, hf[20]), a2[2][2]); SCHED();
  a2[0][3] = mfma16(vf[0][3], __builtin_bit_cast(s16x8, hf[18]), a2[0][3]); SCHED();
  a2[1][3] = mfma16(vf[0][3], __builtin_bit_cast(s16x8, hf[19]), a2[1][3]); SCHED();
  a2[2][3] = mfma16(vf[0][3], __builtin_bit_cast(s16x8, hf[20]), a2[2][3]); SCHED();
  LGKM0();
  asm volatile("s_waitcnt vmcnt(9)" : "+v"(hf[21]), "+v"(hf[22]), "+v"(hf[23]) :: "memory"); SCHED();
  a2[0][0] = mfma16(vf[1][0], __builtin_bit_cast(s16x8, hf[21]), a2[0][0]); SCHED(); RD_V(0, 0, 8); SCHED();
  a2[1][0] = mfma16(vf[1][0], __builtin_bit_cast(s16x8, hf[22]), a2[1][0]); SCHED(); RD_V(0, 1, 8); SCHED();
  a2[2][0] = mfma16(vf[1][0], __builtin_bit_cast(s16x8, hf[23]), a2[2][0]); SCHED(); RD_V(0, 2, 8); SCHED();
  a2[0][1] = mfma16(vf[1][1], __builtin_bit_cast(s16x8, hf[21]), a2[0][1]); SCHED(); RD_V(0, 3, 8); SCHED();
  a2[1][1] = mfma16(vf[1][1], __builtin_bit_cast(s16x8, hf[22]), a2[1][1]); SCHED(); LD_HF(33, 0, 23); SCHED();
  a2[2][1] = mfma16(vf[1][1], __builtin_bit_cast(s16x8, hf[23]), a2[2][1]); SCHED(); LD_HF(34, 1, 23); SCHED();
  a2[0][2] = mfma16(vf[1][2], __builtin_bit_cast(s16x8, hf[21]), a2[0][2]); SCHED(); LD_HF(35, 2, 23); SCHED();
  a2[1][2] = mfma16(vf[1][2], __builtin_bit_cast(s16x8, hf[22]), a2[1][2]); SCHED();
  a2[2][2] = mfma16(vf[1][2], __builtin_bit_cast(s16x8, hf[23]), a2[2][2]); SCHED();
  a2[0][3] = mfma16(vf[1][3], __builtin_bit_cast(s16x8, hf[21]), a2[0][3]); SCHED();
  a2[1][3] = mfma16(vf[1][3], __builtin_bit_cast(s16x8, hf[22]), a2[1][3]); SCHED();
  a2[2][3] = mfma16(vf[1][3], __builtin_bit_cast(s16x8, hf[23]), a2[2][3]); SCHED();
  WAITLBAR();
  asm volatile("s_waitcnt vmcnt(9)" : "+v"(hf[24]), "+v"(hf[25]), "+v"(hf[26]) :: "memory"); SCHED();
  a2[0][0] = mfma16(vf[0][0], __builtin_bit_cast(s16x8, hf[24]), a2[0][0]); SCHED(); RD_V(1, 0, 9); SCHED();
  a2[1][0] = mfma16(vf[0][0], __builtin_bit_cast(s16x8, hf[25]), a2[1][0]); SCHED(); RD_V(1, 1, 9); SCHED();
  a2[2][0] = mfma16(vf[0][0], __builtin_bit_cast(s16x8, hf[26]), a2[2][0]); SCHED(); RD_V(1, 2, 9); SCHED();
  a2[0][1] = mfma16(vf[0][1], __builtin_bit_cast(s16x8, hf[24]), a2[0][1]); SCHED(); RD_V(1, 3, 9); SCHED();
  a2[1][1] = mfma16(vf[0][1], __builtin_bit_cast(s16x8, hf[25]), a2[1][1]); SCHED();
  a2[2][1] = mfma16(vf[0][1], __builtin_bit_cast(s16x8, hf[26]), a2[2][1]); SCHED();
  a2[0][2] = mfma16(vf[0][2], __builtin_bit_cast(s16x8, hf[24]), a2[0][2]); SCHED();
  a2[1][2] = mfma16(vf[0][2], __builtin_bit_cast(s16x8, hf[25]), a2[1][2]); SCHED();
  a2[2][2] = mfma16(vf[0][2], __builtin_bit_cast(s16x8, hf[26]), a2[2][2]); SCHED();
  a2[0][3] = mfma16(vf[0][3], __builtin_bit_cast(s16x8, hf[24]), a2[0][3]); SCHED();
  a2[1][3] = mfma16(vf[0][3], __builtin_bit_cast(s16x8, hf[25]), a2[1][3]); SCHED();
  a2[2][3] = mfma16(vf[0][3], __builtin_bit_cast(s16x8, hf[26]), a2[2][3]); SCHED();
  LGKM0();
  asm volatile("s_waitcnt vmcnt(6)" : "+v"(hf[27]), "+v"(hf[28]), "+v"(hf[29]) :: "memory"); SCHED();
  a2[0][0] = mfma16(vf[1][0], __builtin_bit_cast(s16x8, hf[27]), a2[0][0]); SCHED(); RD_V(0, 0, 10); SCHED();
  a2[1][0] = mfma16(vf[1][0], __builtin_bit_cast(s16x8, hf[28]), a2[1][0]); SCHED(); RD_V(0, 1, 10); SCHED();
  a2[2][0] = mfma16(vf[1][0], __builtin_bit_cast(s16x8, hf[29]), a2[2][0]); SCHED(); RD_V(0, 2, 10); SCHED();
  a2[0][1] = mfma16(vf[1][1], __builtin_bit_cast(s16x8, hf[27]), a2[0][1]); SCHED(); RD_V(0, 3, 10); SCHED();
  a2[1][1] = mfma16(vf[1][1], __builtin_bit_cast(s16x8, hf[28]), a2[1][1]); SCHED();
  a2[2][1] = mfma16(vf[1][1], __builtin_bit_cast(s16x8, hf[29]), a2[2][1]); SCHED();
  a2[0][2] = mfma16(vf[1][2], __builtin_bit_cast(s16x8, hf[27]), a2[0][2]); SCHED();
  a2[1][2] = mfma16(vf[1][2], __builtin_bit_cast(s16x8, hf[28]), a2[1][2]); SCHED();
  a2[2][2] = mfma16(vf[1][2], __builtin_bit_cast(s16x8, hf[29]), a2[2][2]); SCHED();
  a2[0][3] = mfma16(vf[1][3], __builtin_bit_cast(s16x8, hf[27]), a2[0][3]); SCHED();
  a2[1][3] = mfma16(vf[1][3], __builtin_bit_cast(s16x8, hf[28]), a2[1][3]); SCHED();
  a2[2][3] = mfma16(vf[1][3], __builtin_bit_cast(s16x8, hf[29]), a2[2][3]); SCHED();
  WAITLBAR();
  asm volatile("s_waitcnt vmcnt(3)" : "+v"(hf[30]), "+v"(hf[31]), "+v"(hf[32]) :: "memory"); SCHED();
  a2[0][0] = mfma16(vf[0][0], __builtin_bit_cast(s16x8, hf[30]), a2[0][0]); SCHED(); RD_V(1, 0, 0); SCHED();
  a2[1][0] = mfma16(vf[0][0], __builtin_bit_cast(s16x8, hf[31]), a2[1][0]); SCHED(); RD_V(1, 1, 0); SCHED();
  a2[2][0] = mfma16(vf[0][0], __builtin_bit_cast(s16x8, hf[32]), a2[2][0]); SCHED(); RD_V(1, 2, 0); SCHED();
  a2[0][1] = mfma16(vf[0][1], __builtin_bit_cast(s16x8, hf[30]), a2[0][1]); SCHED(); RD_V(1, 3, 0); SCHED();
  a2[1][1] = mfma16(vf[0][1], __builtin_bit_cast(s16x8, hf[31]), a2[1][1]); SCHED();
  a2[2][1] = mfma16(vf[0][1], __builtin_bit_cast(s16x8, hf[32]), a2[2][1]); SCHED();
  a2[0][2] = mfma16(vf[0][2], __builtin_bit_cast(s16x8, hf[30]), a2[0][2]); SCHED();
  a2[1][2] = mfma16(vf[0][2], __builtin_bit_cast(s16x8, hf[31]), a2[1][2]); SCHED();
  a2[2][2] = mfma16(vf[0][2], __builtin_bit_cast(s16x8, hf[32]), a2[2][2]); SCHED();
  a2[0][3] = mfma16(vf[0][3], __builtin_bit_cast(s16x8, hf[30]), a2[0][3]); SCHED();
  a2[1][3] = mfma16(vf[0][3], __builtin_bit_cast(s16x8, hf[31]), a2[1][3]); SCHED();
  a2[2][3] = mfma16(vf[0][3], __builtin_bit_cast(s16x8, hf[32]), a2[2][3]); SCHED();
  LGKM0();
  asm volatile("s_waitcnt vmcnt(0)" : "+v"(hf[33]), "+v"(hf[34]), "+v"(hf[35]) :: "memory"); SCHED();
  a2[0][0] = mfma16(vf[1][0], __builtin_bit_cast(s16x8, hf[33]), a2[0][0]); SCHED();
  a2[1][0] = mfma16(vf[1][0], __builtin_bit_cast(s16x8, hf[34]), a2[1][0]); SCHED();
  a2[2][0] = mfma16(vf[1][0], __builtin_bit_cast(s16x8, hf[35]), a2[2][0]); SCHED();
  a2[0][1] = mfma16(vf[1][1], __builtin_bit_cast(s16x8, hf[33]), a2[0][1]); SCHED();
  a2[1][1] = mfma16(vf[1][1], __builtin_bit_cast(s16x8, hf[34]), a2[1][1]); SCHED();
  a2[2][1] = mfma16(vf[1][1], __builtin_bit_cast(s16x8, hf[35]), a2[2][1]); SCHED();
  a2[0][2] = mfma16(vf[1][2], __builtin_bit_cast(s16x8, hf[33]), a2[0][2]); SCHED();
  a2[1][2] = mfma16(vf[1][2], __builtin_bit_cast(s16x8, hf[34]), a2[1][2]); SCHED();
  a2[2][2] = mfma16(vf[1][2], __builtin_bit_cast(s16x8, hf[35]), a2[2][2]); SCHED();
  a2[0][3] = mfma16(vf[1][3], __builtin_bit_cast(s16x8, hf[33]), a2[0][3]); SCHED();
  a2[1][3] = mfma16(vf[1][3], __builtin_bit_cast(s16x8, hf[34]), a2[1][3]); SCHED();
  a2[2][3] = mfma16(vf[1][3], __builtin_bit_cast(s16x8, hf[35]), a2[2][3]); SCHED();
  WAITLBAR();

  {
    const int wr = 0 + w * 6144 + lane * 16, rd = 0 + (w ^ 1) * 6144 + lane * 16;
#pragma unroll
    for (int r = 0; r < 3; ++r) {
      *(LAS f32x4*)(L + wr + (2 * r) * 1024) = a2[r][2];
      *(LAS f32x4*)(L + wr + (2 * r + 1) * 1024) = a2[r][3];
    }
    b2v = *(const GAS u32x2*)(b2p + (64 * line + 32 * kh2 + 4 * g) * 2);
    u32x2 b2w = *(const GAS u32x2*)(b2p + (64 * line + 32 * kh2 + 16 + 4 * g) * 2);
    WAITLBAR();

    const float bv[2][4] = {{__builtin_bit_cast(float, b2v.x << 16), __builtin_bit_cast(float, b2v.x & 0xffff0000u),
                             __builtin_bit_cast(float, b2v.y << 16), __builtin_bit_cast(float, b2v.y & 0xffff0000u)},
                            {__builtin_bit_cast(float, b2w.x << 16), __builtin_bit_cast(float, b2w.x & 0xffff0000u),
                             __builtin_bit_cast(float, b2w.y << 16), __builtin_bit_cast(float, b2w.y & 0xffff0000u)}};
#pragma unroll
    for (int r = 0; r < 3; ++r) {
      const int rt = 6 * rb2 + 3 * mh2 + r;
#pragma unroll
      for (int t = 0; t < 2; ++t) {
        const f32x4 s = a2[r][t] + *(const LAS f32x4*)(L + rd + (2 * r + t) * 1024);
        if (rt < 128) {
          u32x2 ov = {pack2(s[0] + bv[t][0], s[1] + bv[t][1]), pack2(s[2] + bv[t][2], s[3] + bv[t][3])};
          *(GAS u32x2*)(outp + ((size_t)(16 * rt + m) * 640 + 64 * line + 32 * kh2 + 16 * t + 4 * g) * 2) = ov;
        }
      }
    }
  }

}

extern "C" void kernel_launch(void* const* d_in, const int* in_sizes, int n_in, void* d_out, int out_size, void* d_ws,
                              size_t ws_size, hipStream_t stream) {
  (void)hipFuncSetAttribute((const void*)mixer_fused, hipFuncAttributeMaxDynamicSharedMemorySize, LDS_BYTES);
  mixer_fused<<<256, 512, LDS_BYTES, stream>>>((const __hip_bfloat16*)d_in[0], (const __hip_bfloat16*)d_in[1], (const __hip_bfloat16*)d_in[2],
                                               (const __hip_bfloat16*)d_in[3], (const __hip_bfloat16*)d_in[4], (__hip_bfloat16*)d_out,
                                               (char*)d_ws);
}
```
